```python
import torch

def _softmax(x, axis=-1):
    return torch.softmax(x, dim=axis)
def _gelu(x):
    return torch.nn.functional.gelu(x, approximate="tanh")
def _silu(x):
    return torch.nn.functional.silu(x)
def _sigmoid(x):
    return torch.sigmoid(x)
def _layernorm(x, axis=-1, eps=1e-5):
    m = x.mean(dim=axis, keepdim=True)
    v = x.var(dim=axis, keepdim=True, unbiased=False)
    return (x - m) / torch.sqrt(v + eps)
def _scatter_add(tgt, idx, src, axis=0):
    return tgt.clone().index_add_(axis, idx, src)
def _segment_sum(src, seg, n):
    out = torch.zeros((n,) + src.shape[1:], dtype=src.dtype)
    return out.index_add_(0, seg, src)
def _pad(x, pw):
    flat = tuple(v for pair in reversed(pw) for v in pair)
    return torch.nn.functional.pad(x, flat)

kernel_name = 'gated_mix__ssm_conv_s5__glu_gated_swiglu__prompt'

def setup_inputs():

    _w = (lambda a: (lambda t: t.to(torch.bfloat16) if t.is_floating_point() else t)(torch.as_tensor(a)))
    def _A_setup():
        B, S, D = 1, 2048, 448
        P = D
        Lambda_re = -0.5 * (lambda _x: torch.exp(torch.as_tensor(_x)))((lambda *s: torch.randn(*s, dtype=torch.bfloat16) * 0.2)(P))
        Lambda_im = (lambda *s: torch.randn(*s, dtype=torch.bfloat16) * 0.4)(P) * 1.0
        B_re = (lambda *s: torch.randn(*s, dtype=torch.bfloat16) * 0.2)(P, D) * (D ** -0.5)
        B_im = (lambda *s: torch.randn(*s, dtype=torch.bfloat16) * 0.2)(P, D) * (D ** -0.5)
        C_re = (lambda *s: torch.randn(*s, dtype=torch.bfloat16) * 0.2)(D, P) * (P ** -0.5)
        C_im = (lambda *s: torch.randn(*s, dtype=torch.bfloat16) * 0.2)(D, P) * (P ** -0.5)
        log_dt = (lambda *s: torch.randn(*s, dtype=torch.bfloat16) * 0.2)(P) * 0.3 - 2.0
        Dskip = (lambda *s: torch.randn(*s, dtype=torch.bfloat16) * 0.05)(D)
        return {
            "x": _w((lambda *s: torch.randn(*s, dtype=torch.bfloat16) * 0.05)(B, S, D)),
            "Lambda_re": _w(Lambda_re),
            "Lambda_im": _w(Lambda_im),
            "B_re": _w(B_re),
            "B_im": _w(B_im),
            "C_re": _w(C_re),
            "C_im": _w(C_im),
            "log_dt": _w(log_dt),
            "Dskip": _w(Dskip),
        }
    _A_in = _A_setup()

    def _B_setup():
        B, S, D, F = 1, 2048, 448, 1792
        return {
            "x": _w((lambda *s: torch.randn(*s, dtype=torch.bfloat16) * 0.05)(B, S, D)),
            "w1": _w((lambda *s: torch.randn(*s, dtype=torch.bfloat16) * 0.8)(D, F) * (2.0 / D) ** 0.5),
            "w3": _w((lambda *s: torch.randn(*s, dtype=torch.bfloat16) * 0.8)(D, F) * (2.0 / D) ** 0.5),
            "w2": _w((lambda *s: torch.randn(*s, dtype=torch.bfloat16) * 0.8)(F, D) * (2.0 / F) ** 0.5),
        }
    _B_in = _B_setup()

    return {
        "A_x": _A_in["x"],
        "A_Lambda_re": _A_in["Lambda_re"],
        "A_Lambda_im": _A_in["Lambda_im"],
        "A_B_re": _A_in["B_re"],
        "A_B_im": _A_in["B_im"],
        "A_C_re": _A_in["C_re"],
        "A_C_im": _A_in["C_im"],
        "A_log_dt": _A_in["log_dt"],
        "A_Dskip": _A_in["Dskip"],
        "B_x": _B_in["x"],
        "B_w1": _B_in["w1"],
        "B_w3": _B_in["w3"],
        "B_w2": _B_in["w2"],
        "gate_w": _w((lambda *s: torch.randn(*s, dtype=torch.bfloat16) * 0.05)(448) * 16.0),
    }

def reference(A_x, A_Lambda_re, A_Lambda_im, A_B_re, A_B_im, A_C_re, A_C_im, A_log_dt, A_Dskip, B_x, B_w1, B_w3, B_w2, gate_w):
    def _A_ref(x, Lambda_re, Lambda_im, B_re, B_im, C_re, C_im, log_dt, Dskip):
        Bsz, L, H = x.shape
        Lam = Lambda_re + 1j * Lambda_im
        Bmat = B_re + 1j * B_im
        Cmat = C_re + 1j * C_im
        dt = torch.exp(log_dt)
        Lam_bar = torch.exp(dt * Lam)
        B_bar = ((Lam_bar - 1.0) / Lam)[:, None] * Bmat
        Bu = torch.einsum("bld,pd->blp", x + 0j, B_bar)
        h = torch.zeros_like(Bu[:, 0, :])
        hs = []
        for t in range(L):
            h = Lam_bar[None, :] * h + Bu[:, t, :]
            hs.append(h)
        hseq = torch.stack(hs, 1)
        y = 2.0 * torch.einsum("blp,dp->bld", hseq, Cmat).real + x * Dskip[None, None, :]
        return y

    def _B_ref(x, w1, w3, w2):
        gate = torch.einsum("bsd,df->bsf", x, w1)
        up = torch.einsum("bsd,df->bsf", x, w3)
        h = _silu(gate) * up
        out = torch.einsum("bsf,fd->bsd", h, w2)
        return out

    x = A_x
    g = _sigmoid((x * gate_w).sum(-1, keepdims=True))
    a = _A_ref(x, A_Lambda_re, A_Lambda_im, A_B_re, A_B_im, A_C_re, A_C_im, A_log_dt, A_Dskip)
    b = _B_ref(x, B_w1, B_w3, B_w2)
    return g * a + (1 - g) * b
```

```cpp
#include <hip/hip_runtime.h>
#include <hip/hip_bf16.h>
typedef unsigned short u16;
__device__ __forceinline__ float bf2f(u16 v){ return __uint_as_float(((unsigned)v)<<16); }
__device__ __forceinline__ float rbf(float x){ unsigned u=__float_as_uint(x); u += 0x7fffu + ((u>>16)&1u); return __uint_as_float(u & 0xffff0000u); }
constexpr int L=2048, D=448, P=448, F=1792;

__global__ void k_bu(const u16* x, const u16* Bre, const u16* Bim, const u16* lre, const u16* lim, const u16* ldt, float2* Bu){
  int t = blockIdx.x; int p = threadIdx.x;
  if (p>=P) return;
  float sr=0, si=0;
  for (int d=0; d<D; ++d){ float xv=bf2f(x[t*D+d]); sr += xv*bf2f(Bre[p*D+d]); si += xv*bf2f(Bim[p*D+d]); }
  float dt = rbf(expf(bf2f(ldt[p])));
  float a = bf2f(lre[p]), b = bf2f(lim[p]);
  float er = expf(dt*a); float br = er*cosf(dt*b), bi = er*sinf(dt*b);
  float nr = br-1.f, ni = bi; float den = a*a+b*b;
  float cr = (nr*a+ni*b)/den, ci = (ni*a-nr*b)/den;
  Bu[t*P+p] = make_float2(cr*sr-ci*si, cr*si+ci*sr);
}
__global__ void k_scan(float2* Bu, const u16* lre, const u16* lim, const u16* ldt){
  int p = blockIdx.x*64+threadIdx.x;
  float dt = rbf(expf(bf2f(ldt[p])));
  float a = bf2f(lre[p]), b = bf2f(lim[p]);
  float er = expf(dt*a); float br = er*cosf(dt*b), bi = er*sinf(dt*b);
  float hr=0, hi=0;
  for (int t=0;t<L;++t){ float2 u=Bu[t*P+p]; float nr = br*hr-bi*hi+u.x, ni = br*hi+bi*hr+u.y; hr=nr; hi=ni; Bu[t*P+p]=make_float2(hr,hi);}
}
__global__ void k_h(const u16* x, const u16* w1, const u16* w3, float* H){
  int t = blockIdx.x;
  for (int f=threadIdx.x; f<F; f+=blockDim.x){
    float g=0,u=0;
    for (int d=0; d<D; ++d){ float xv=bf2f(x[t*D+d]); g += xv*bf2f(w1[d*F+f]); u += xv*bf2f(w3[d*F+f]); }
    H[t*F+f] = rbf(g/(1.f+expf(-g))*u);
  }
}
__global__ void k_out(const u16* x, const float2* h, const float* H, const u16* Cre, const u16* Cim, const u16* Dsk, const u16* w2, const u16* gw, float* out){
  int t = blockIdx.x; int d = threadIdx.x;
  if (d>=D) return;
  float gs=0; for (int k=0;k<D;++k) gs += bf2f(x[t*D+k])*bf2f(gw[k]);
  float g = 1.f/(1.f+expf(-gs));
  float y=0; for (int p=0;p<P;++p){ float2 hv=h[t*P+p]; y += rbf(hv.x)*bf2f(Cre[d*P+p]) - rbf(hv.y)*bf2f(Cim[d*P+p]); }
  float a = 2.f*y + bf2f(x[t*D+d])*bf2f(Dsk[d]);
  float b=0; for (int f=0;f<F;++f) b += H[t*F+f]*bf2f(w2[f*D+d]);
  out[t*D+d] = g*a + (1.f-g)*b;
}
extern "C" void kernel_launch(void* const* d_in, const int* in_sizes, int n_in, void* d_out, int out_size, void* d_ws, size_t ws_size, hipStream_t stream) {
  const u16* x=(const u16*)d_in[0]; const u16* lre=(const u16*)d_in[1]; const u16* lim=(const u16*)d_in[2];
  const u16* Bre=(const u16*)d_in[3]; const u16* Bim=(const u16*)d_in[4]; const u16* Cre=(const u16*)d_in[5]; const u16* Cim=(const u16*)d_in[6];
  const u16* ldt=(const u16*)d_in[7]; const u16* Dsk=(const u16*)d_in[8];
  const u16* w1=(const u16*)d_in[10]; const u16* w3=(const u16*)d_in[11]; const u16* w2=(const u16*)d_in[12]; const u16* gw=(const u16*)d_in[13];
  float2* Bu=(float2*)d_ws; float* H=(float*)((char*)d_ws + (size_t)L*P*8);
  k_bu<<<L,448,0,stream>>>(x,Bre,Bim,lre,lim,ldt,Bu);
  k_scan<<<P/64,64,0,stream>>>(Bu,lre,lim,ldt);
  k_h<<<L,256,0,stream>>>(x,w1,w3,H);
  k_out<<<L,448,0,stream>>>(x,Bu,H,Cre,Cim,Dsk,w2,gw,(float*)d_out);
}
```

```cpp
#include <hip/hip_runtime.h>
#pragma clang diagnostic ignored "-Wint-to-pointer-cast"
typedef __bf16 bf16x8 __attribute__((ext_vector_type(8)));
typedef __bf16 bf16x4 __attribute__((ext_vector_type(4)));
typedef __bf16 bf16x2 __attribute__((ext_vector_type(2)));
typedef float f32x16 __attribute__((ext_vector_type(16)));
typedef float f32x4 __attribute__((ext_vector_type(4)));
typedef float f32x2 __attribute__((ext_vector_type(2)));
typedef unsigned u32x4 __attribute__((ext_vector_type(4)));
typedef unsigned u32x2 __attribute__((ext_vector_type(2)));
#define LAS __attribute__((address_space(3)))
#define CAS __attribute__((address_space(4)))
#define SB __builtin_amdgcn_sched_barrier(0)
#define RFL(x) __builtin_amdgcn_readfirstlane(x)
#define MFMA(a, b, c) __builtin_amdgcn_mfma_f32_32x32x16_bf16(a, b, c, 0, 0, 0)
#define TRR(p) __builtin_amdgcn_ds_read_tr16_b64_v4bf16((LAS bf16x4*)(p))
#define RD128(p) (*(LAS bf16x8*)(p))
#define CAT(a, b) __builtin_shufflevector(a, b, 0, 1, 2, 3, 4, 5, 6, 7)
#define BC(a) __builtin_bit_cast(bf16x8, a)
#define UC(a) __builtin_bit_cast(u32x4, a)
#define Q4(a, i) __builtin_shufflevector(a, a, 4 * (i), 4 * (i) + 1, 4 * (i) + 2, 4 * (i) + 3)
#define PK(a, b) __builtin_bit_cast(unsigned, __builtin_convertvector(f32x2{a, b}, bf16x2))
#define PIN1(a) asm volatile("" : "+v"(a))
#define PIN2(a, b) asm volatile("" : "+v"(a), "+v"(b))
#define PIN3(a, b, c) asm volatile("" : "+v"(a), "+v"(b), "+v"(c))
#define PIN4(a, b, c, d) asm volatile("" : "+v"(a), "+v"(b), "+v"(c), "+v"(d))
#define SWAP16(a, b, x, y) do { auto r_ = __builtin_amdgcn_permlane16_swap(__float_as_uint(a), __float_as_uint(b), false, false); x = __uint_as_float(r_[0]); y = __uint_as_float(r_[1]); } while (0)
#define SWAP32(a, b, x, y) do { auto r_ = __builtin_amdgcn_permlane32_swap(__float_as_uint(a), __float_as_uint(b), false, false); x = __uint_as_float(r_[0]); y = __uint_as_float(r_[1]); } while (0)
#define SWAP8(a) asm volatile("v_permlane16_swap_b32 v2, v10\n\tv_permlane16_swap_b32 v3, v11\n\tv_permlane16_swap_b32 v4, v12\n\tv_permlane16_swap_b32 v5, v13\n\t" \
    "v_permlane16_swap_b32 v6, v14\n\tv_permlane16_swap_b32 v7, v15\n\tv_permlane16_swap_b32 v8, v16\n\tv_permlane16_swap_b32 v9, v17" : "+{v[2:17]}"(a)); \
  const float v0r = a[0], v1r = a[1], v2r = a[2], v3r = a[3], v4r = a[4], v5r = a[5], v6r = a[6], v7r = a[7], v0i = a[8], v1i = a[9], v2i = a[10], v3i = a[11], v4i = a[12], v5i = a[13], v6i = a[14], v7i = a[15]
#define DMAI(rs, vo, so, lo) do { int t_; asm volatile("s_add_u32 m0, %1, %2\n\ts_mov_b32 %0, %5\n\tbuffer_load_dwordx4 %3, %4, %0 offen lds" : "=&s"(t_) : "s"(ldsw), "i"(lo), "v"(vo), "s"(rs), "i"(so) : "scc"); } while (0)
#define DMAK(rs, vo, op, k, c, lo) do { int t_; asm volatile("s_add_u32 m0, %1, %2\n\t" op " %0, %5, %6\n\tbuffer_load_dwordx4 %3, %4, %0 offen lds" : "=&s"(t_) : "s"(ldsw), "i"(lo), "v"(vo), "s"(rs), "s"(k), "i"(c) : "scc"); } while (0)
#define LDV(d, vo, so, e) asm volatile("buffer_load_dwordx4 %0, %1, %2, %3 offen offset:%4 sc1" : "=v"(d) : "v"(vo), "s"(rsws), "s"(so), "i"((e) * 1024))
#define LD16(d, rs, vo) asm volatile("buffer_load_ushort %0, %1, %2, 0 offen" : "=v"(d) : "v"(vo), "s"(rs))
#define LDE(d) asm volatile("buffer_load_dwordx2 %0, off, %1, 0 sc1" : "=v"(d) : "s"(rsws))
#define LD128(d, vo, so) asm volatile("buffer_load_dwordx4 %0, %1, %2, %3 offen sc1" : "=v"(d) : "v"(vo), "s"(rsws), "s"(so))
#define ST128(d, vo, so) asm volatile("buffer_store_dwordx4 %0, %1, %2, %3 offen sc1\n\ts_nop 1" :: "v"(d), "v"(vo), "s"(rsws), "s"(so) : "memory")
#define LDA(d, so, e) asm volatile("buffer_load_dwordx4 %0, %1, %2, %3 offen offset:%4 sc1" : "=v"(d) : "v"(l16), "s"(rsws), "s"(so), "i"((e) * 1024))
#define LDR(d, o) asm volatile("buffer_load_dwordx4 %0, %1, %2, %3 offen offset:%4 sc1" : "=v"(d) : "v"(vrm), "s"(rsws), "s"(plr), "i"(o))
#define STFLAG(so) do { u32x2 tg_ = {tagl, tagh}; asm volatile("s_mov_b64 exec, 1\n\tbuffer_store_dwordx2 %0, off, %1, %2 sc1\n\ts_mov_b64 exec, -1\n\ts_nop 0" :: "v"(tg_), "s"(rsws), "s"(so) : "memory"); } while (0)
#define LDFLAG(d, vo, so) asm volatile("buffer_load_dwordx2 %0, %1, %2, %3 offen sc1" : "=v"(d) : "v"(vo), "s"(rsws), "s"(so))
#define LDX(d, rs, vo, so) asm volatile("buffer_load_dwordx2 %0, %1, %2, %3 offen" : "=v"(d) : "v"(vo), "s"(rs), "s"(so))
#define STX(d, so, e) asm volatile("buffer_store_dwordx4 %0, %1, %2, %3 offen offset:%4 sc1\n\ts_nop 1" :: "v"(d), "v"(l16), "s"(rsws), "s"(so), "i"((e) * 1024) : "memory")
#define ANY(c) (__builtin_amdgcn_ballot_w64(c) != 0ull)
#define U64(a) __builtin_bit_cast(unsigned long long, a)
#define PFL(rs, vo, so) asm volatile("buffer_load_dword %0, %1, %2, %3 offen" : "+v"(pfd) : "v"(vo), "s"(rs), "s"(so))
#define PFD(rs, vo) asm volatile("s_mov_b32 m0, %0\n\ts_nop 0\n\tbuffer_load_dword %1, %2, 0 offen lds" :: "s"(sb + LM + 4096), "v"(vo), "s"(rs))
#define WROW(a, k, o, ws) asm volatile(ws "\n\tds_write_addtid_b32 %0 offset:%5\n\tds_write_addtid_b32 %1 offset:%6\n\tds_write_addtid_b32 %2 offset:%7\n\tds_write_addtid_b32 %3 offset:%8" :: "v"(a[4 * (k)]), "v"(a[4 * (k) + 1]), "v"(a[4 * (k) + 2]), "v"(a[4 * (k) + 3]), "s"(wm0), "i"(o), "i"((o) + 256), "i"((o) + 512), "i"((o) + 768) : "memory")
#define MFSEL(c, a, b) asm volatile("s_cmp_lg_u32 %3, 0\n\ts_cbranch_scc1 1f\n\tv_mfma_f32_32x32x16_bf16 %0, %1, %2, 0\n\ts_branch 2f\n1:\n\tv_mfma_f32_32x32x16_bf16 %0, %1, %2, %0\n2:" : "+v"(c) : "v"(a), "v"(b), "s"(kpo) : "scc")
#define SPINMAX 200000
#define OOB 0x7ffffff0

#define WS_FLAG 4096
#define WS_SG   131072
#define WS_GG   262144
#define WS_H    1048576
#define WS_P    (WS_H + 16 * 3 * 4 * 56 * 1024)
#define WS_END  (WS_P + 16 * 7 * 2 * 8 * 2048)
#define LM 155648

#define BA(i) const void* p##i, unsigned n##i, unsigned f##i

__device__ __forceinline__ float rbf(float x) { unsigned u = __float_as_uint(x); u += 0x7fffu + ((u >> 16) & 1u); return __uint_as_float(u & 0xffff0000u); }
__device__ __forceinline__ float blo(unsigned u) { return __uint_as_float(u << 16); }
__device__ __forceinline__ float bhi(unsigned u) { return __uint_as_float(u & 0xffff0000u); }
__device__ __forceinline__ float fmul1(float a, float b) { float r; asm("v_mul_f32 %0, %1, %2" : "=v"(r) : "v"(a), "v"(b)); return r; }
__device__ __forceinline__ float fadd1(float a, float b) { float r; asm("v_add_f32 %0, %1, %2" : "=v"(r) : "v"(a), "v"(b)); return r; }
__device__ __forceinline__ int perm32(int i) { return 16 * ((i >> 2) & 1) + (i & 3) + 4 * (i >> 3); }

__global__ __launch_bounds__(512, 2) void fused_kernel(BA(0), BA(1), BA(2), BA(3), BA(4), BA(5), BA(6), BA(7), BA(8), BA(9), BA(10), BA(11), BA(12), BA(13), BA(14)) {
  __shared__ __attribute__((aligned(1024))) char smem[163840];
  const CAS u32x4* ka = (const CAS u32x4*)__builtin_amdgcn_kernarg_segment_ptr();
  const int tid = threadIdx.x, lane = tid & 63;
  const int w = RFL(tid >> 6);
  const int xcd = blockIdx.x & 7, rk = blockIdx.x >> 3, rh = xcd & 1;
  const u32x4 rsx = ka[0], rsw1 = ka[1], rsw3 = ka[2], rsws = ka[3];
  const u32x4 rsl0 = ka[4], rsl1 = ka[5], rsl2 = ka[6], rsw2 = ka[7];
  const int wd = (w + (rk*4)) & 7;
  u32x4 rb0_ = ka[8], rb1_ = ka[9];
  const u32x4 rscr = ka[10], rsci = ka[11];
  { unsigned long long pc_; asm volatile("s_getpc_b64 %0\n\ts_mov_b32 m0, %2\n\ts_cmp_lt_u32 %3, 3\n\ts_cselect_b64 exec, -1, 0\n\tglobal_load_lds_dword %1, %0\n\ts_mov_b64 exec, -1" : "=&s"(pc_) : "v"(min(tid, 157) * 128), "s"((unsigned)(size_t)(LAS char*)smem + LM + 4096), "s"((rk >= 28 || rk == 0 ? w : 8)) : "scc"); }
  if (__builtin_expect(rk >= 28, 0)) {
    const u32x4 rsg = ka[14], rsd = ka[12];
    const int t0 = (4 * (xcd >> 1) + rk - 28) * 128 + rh * 64;
    u32x2 pE; LDE(pE);
    __builtin_amdgcn_s_sleep(10);
    const int t = t0 + (tid >> 3), c = tid & 7;
    u32x4 a[7], b[7];
#pragma unroll
    for (int k = 0; k < 7; ++k) {
      asm volatile("buffer_load_dwordx4 %0, %1, %2, 0 offen offset:%3" : "=v"(a[k]) : "v"(t * 896 + c * 16), "s"(rsx), "i"(k * 128));
      asm volatile("buffer_load_dwordx4 %0, %1, %2, 0 offen offset:%3" : "=v"(b[k]) : "v"(c * 16), "s"(rsg), "i"(k * 128));
    }
    const int hw = w * 4 + rk - 28, xq = xcd >> 1;
    unsigned pfd = 0;
#define PFV(i, o) { const int j = (i) / 14, r = (i) % 14; const u32x4 rv = r < 7 ? rsw1 : rsw3; PFL(rv, (lane * 3584 + (o) + 896 * rh + (r % 7) * 128) | ob_, j * 229376); }
#define PFC(i) { const int vo = ((((i) % 28) * 16 + (lane >> 2)) * 896 + 384 * rh + (lane & 3) * 128) | ob_; const u32x4 rv = (i) < 28 ? rscr : rsci; PFL(rv, vo, 0); }
#define PFB(i) { const u32x4 rb = ka[8 + ((i) >= 25)]; PFL(rb, (224 * 896 * rh + ((i) % 25) * 8192 + lane * 128) | ob_, 0); }
#define PFP(i) PFL(rsw2, (lane * 128) | ob_, 448 * rh * 896 + (i) * 8192);
#define PFQ(i) PFL(rsw2, (lane * 128) | ob_, (896 + 448 * rh) * 896 + (i) * 8192);
#define PFR(M, lo, hi, sh, id, s0, s1, h0) for (int s_ = s0; s_ < s1; ++s_) { const int j_ = (lo) + (((hw + 32 - (h0)) & 31) << sh) + ((id + s_) & ((1 << sh) - 1)); const int ob_ = j_ < (hi) ? 0 : OOB, i_ = j_ < (hi) ? j_ : 0; M }
    PFL(rsd, hw == 31 ? lane * 128 : OOB, 0);
    asm volatile("s_waitcnt vmcnt(1)" : "+v"(pE), "+v"(a[0]), "+v"(a[1]), "+v"(a[2]), "+v"(a[3]), "+v"(a[4]), "+v"(a[5]), "+v"(a[6]),
        "+v"(b[0]), "+v"(b[1]), "+v"(b[2]), "+v"(b[3]), "+v"(b[4]), "+v"(b[5]), "+v"(b[6]));
    float s = 0.f;
#pragma unroll
    for (int k = 0; k < 7; ++k)
#pragma unroll
      for (int j = 0; j < 4; ++j) s += blo(a[k][j]) * blo(b[k][j]) + bhi(a[k][j]) * bhi(b[k][j]);
    s += __shfl_xor(s, 1); s += __shfl_xor(s, 2); s += __shfl_xor(s, 4);
    const float g = 1.f / (1.f + expf(-s));
    float ci = 1.f + expf(s);
    ci = ci > 1e30f ? 1e30f : ci;
    unsigned long long tag64 = ((unsigned long long)RFL(pE[1]) << 32 | (unsigned)RFL(pE[0])) * 6364136223846793005ull + 1442695040888963407ull;
    const unsigned tagl = (unsigned)tag64, tagh = (unsigned)(tag64 >> 32);
    if (c == 0) { u32x4 gr = {__float_as_uint(g), tagl, __float_as_uint(ci), tagh}; ST128(gr, t * 16, WS_GG); }
    PFR(PFV(i_, 1792), 0, 98, 2, xq, 0, 1, 0) PFR(PFC(i_), 0, 56, 2, xq, 0, 1, 25) PFR(PFP(i_), 0, 49, 2, xq, 0, 1, 7) PFR(PFQ(i_), 0, 49, 2, xq, 0, 1, 20)
    asm volatile("s_waitcnt vmcnt(0)" : "+v"(pfd));
    if (blockIdx.x == 255 && w == 0) {
      const int vo = lane < 32 ? WS_GG + lane * 1024 : WS_FLAG + 15 * 4096 + 1024 + ((lane - 32) % 7) * 16;
      for (int sp = 0; sp < SPINMAX; ++sp) {
        u32x4 v; LD128(v, vo, 0); asm volatile("s_waitcnt vmcnt(0)" : "+v"(v));
        if (__all(lane < 32 ? (v[1] == tagl && v[3] == tagh) : (v[0] == tagl && v[1] == tagh && v[2] == tagl && v[3] == tagh))) break;
        __builtin_amdgcn_s_sleep(16);
      }
      if (lane == 0) { u32x2 tg = {tagl, tagh}; asm volatile("buffer_store_dwordx2 %0, off, %1, 0 sc1" :: "v"(tg), "s"(rsws) : "memory"); }
    }
    __builtin_amdgcn_endpgm();
  }
  const int m = 4 * (xcd >> 1) + rk / 7, ct = rk % 7, n = 7 * rh + ct;
  const int mh = w & 1, mu = w >> 2, mi = mh + 2 * mu, ni = (w >> 1) & 1;
  const int l3 = lane >> 3, l7 = lane & 7, q = lane >> 4, st = lane & 15, i32 = lane & 31, h = lane >> 5;
  const unsigned sb = (unsigned)(size_t)(LAS char*)smem;
  const unsigned ldsw = (unsigned)(size_t)(LAS char*)smem + wd * 1024;
  const int fx = 16 * (l7 ^ ((4 * wd + q) & 7)), fw = 16 * (l7 ^ ((q & 1) << 2));
  int vx = (128 * m + 32 * (wd >> 2) + perm32(8 * (wd & 3) + l3)) * 896 + fx;
  int vb = (32 * n + 16 * (wd >> 2) + 8 * (wd & 1) + l3) * 896 + fx;
  int vw = (8 * wd + l3) * 3584 + 128 * n + fw;
  int vp = (32 * n + 16 * ni + st) * 2;
  asm volatile("" : "+v"(vx), "+v"(vb), "+v"(vw), "+v"(vp), "+s"(rb0_), "+s"(rb1_));
  SB;
  const u32x4 rsb = {((wd >> 1) & 1) ? rb1_[0] : rb0_[0], ((wd >> 1) & 1) ? rb1_[1] : rb0_[1], rb0_[2], rb0_[3]};
  const f32x16 zero16 = {0.f, 0.f, 0.f, 0.f, 0.f, 0.f, 0.f, 0.f, 0.f, 0.f, 0.f, 0.f, 0.f, 0.f, 0.f, 0.f};
  f32x16 sacc, ag0, au0, ag1, au1, ap0, ap1, an0, an1, aq0, aq1;
#define SETUPC \
  const unsigned rdp = i32 * 128 + 16 * (h ^ ((i32 >> 1) & 7)); \
  const int c4 = lane & 3, r4 = (lane >> 2) & 3, b16 = (lane >> 4) & 1; \
  const unsigned wbz = sb + (8 * h + r4) * 128 + ((64 * ni + 32 * b16 + 16 * (c4 & 1) + 8 * (c4 >> 1)) ^ ((r4 >> 1) << 6)); \
  const int ex = m - 1 - (4 * mi + q); \
  const bool keep = mu == rh;
  {
    unsigned plre; LD16(plre, rsl0, vp);
    unsigned plim; LD16(plim, rsl1, vp);
    unsigned pldt; LD16(pldt, rsl2, vp);
    u32x2 pE; LDE(pE);
    DMAI(rsx, vx, 0, 0);
    DMAI(rsx, vx, 57344, 8192);
    DMAI(rsb, vb, 0, 65536);
    DMAI(rsw1, vw, 0, 139264);
    DMAI(rsw3, vw, 0, 147456);
    PFD((w & 1) ? rsw3 : rsw1, lane < 6 ? (64 + 24*m + 6*(w >> 1) + lane)*3584 + 128*n : OOB);
    PFD(rsb, lane < 6 && wd < 4 ? (32*n + 2*m + (wd & 1))*896 + 128 + 128*lane : OOB);
    SB;
    asm volatile("" : "+s"(ka));
    const u32x4 rsd = ka[12], rso = ka[13];
    SETUPC
    unsigned xb0 = (sb + mi*4096 + (rdp ^ 0)) + 0; PIN1(xb0);
    unsigned xb1 = (sb + mi*4096 + (rdp ^ 32)) + 0; PIN1(xb1);
    unsigned xb2 = (sb + mi*4096 + (rdp ^ 64)) + 0; PIN1(xb2);
    unsigned xb3 = (sb + mi*4096 + (rdp ^ 96)) + 0; PIN1(xb3);
    unsigned bb0 = (sb + ni*4096 + (rdp ^ 0)) + 65536; PIN1(bb0);
    unsigned bb1 = (sb + ni*4096 + (rdp ^ 32)) + 65536; PIN1(bb1);
    unsigned bb2 = (sb + ni*4096 + (rdp ^ 64)) + 65536; PIN1(bb2);
    unsigned bb3 = (sb + ni*4096 + (rdp ^ 96)) + 65536; PIN1(bb3);
    unsigned wb = wbz + 139264; PIN1(wb);
    SB;
    int kq0 = ct * 256; int kq1 = ct >= 6 ? kq0 - 1536 : kq0 + 256; int kq2 = ct >= 5 ? kq0 - 1280 : kq0 + 512; int kq3 = ct >= 4 ? kq0 - 1024 : kq0 + 768; asm volatile("" : "+s"(kq0), "+s"(kq1), "+s"(kq2), "+s"(kq3));
    int kq4 = ct >= 3 ? kq0 - 768 : kq0 + 1024; int kq5 = ct >= 2 ? kq0 - 512 : kq0 + 1280; int kq6 = ct >= 1 ? kq0 - 256 : kq0 + 1536; asm volatile("" : "+s"(kq4), "+s"(kq5), "+s"(kq6));
    int sgo = WS_SG + (m * 448 + 32 * n + 16 * ni) * 16, sgi = WS_SG + (4 * mi * 448 + 32 * n + 16 * ni) * 16, ggo = WS_GG + (128 * m + 32 * mi) * 16; asm volatile("" : "+s"(sgo), "+s"(sgi), "+s"(ggo));
    int hso = WS_H + ((m * 12 + mi) * 56 + 4 * n + 2 * ni) * 1024, hl1 = hso + 229376 - kq0 * 16; asm volatile("" : "+s"(hso), "+s"(hl1));
    int fpl = WS_FLAG + 1024 + m * 4096 + (2 * mi + ni) * 128 + rh * 56, fso = fpl - 1024 + ct * 8, pli = WS_P + (((m * 7 + ct) * 2 + rh) * 8 + w) * 2048; asm volatile("" : "+s"(fpl), "+s"(fso), "+s"(pli));
    asm volatile("s_waitcnt vmcnt(7)" : "+v"(plre), "+v"(plim), "+v"(pldt), "+v"(pE) : "s"(rsd), "s"(rso));
    unsigned long long tag64 = ((unsigned long long)RFL(pE[1]) << 32 | (unsigned)RFL(pE[0])) * 6364136223846793005ull + 1442695040888963407ull; unsigned tagl = (unsigned)tag64, tagh = (unsigned)(tag64 >> 32);
    float la = __uint_as_float(plre << 16), lb = __uint_as_float(plim << 16); float dt = expf(__uint_as_float(pldt << 16));
    PIN3(la,lb,dt);
    dt = rbf(dt); float aa = dt*la, bb = dt*lb;
    PIN2(aa,bb);
    float er = expf(aa);
    PIN1(er);
    float kq = rintf(bb*0.636619772f); float rr = fmaf(kq,-1.5707963705062866f,bb); rr = fmaf(kq,4.371138828673793e-08f,rr); rr = fmaf(kq,1.7151245100058819e-15f,rr); int kn = (int)kq;
    PIN2(rr,kn);
    float zz = rr*rr; float sn = fmaf(fmaf(fmaf(-1.9515295891e-4f,zz,8.3321608736e-3f),zz,-1.6666654611e-1f)*zz,rr,rr);
    PIN2(zz,sn);
    float cs = fmaf(fmaf(fmaf(2.443315711809948e-5f,zz,-1.388731625493765e-3f),zz,4.166664568298827e-2f)*zz,zz,fmaf(-0.5f,zz,1.f));
    PIN1(cs);
    float s2 = (kn&1) ? cs : sn, c2 = (kn&1) ? sn : cs;
    PIN2(s2,c2);
    s2 = (kn&2) ? -s2 : s2; c2 = ((kn+1)&2) ? -c2 : c2;
    PIN2(s2,c2);
    float Lr = er*c2, Li = er*s2; float nr = Lr-1.f; float den = fmaf(la,la,lb*lb);
    PIN4(Lr,Li,nr,den);
    float inv = 2.f/den;
    PIN1(inv);
    float Kr = fmaf(nr,la,Li*lb)*inv, Ki = fmaf(Li,la,-(nr*lb))*inv;
    PIN2(Kr,Ki);
    SB;
    DMAI(rsx, vx, 128, 16384);
    DMAI(rsx, vx, 57472, 24576);
    DMAI(rsb, vb, 128, 73728);
    DMAI(rsx, vx, 256, 32768);
    DMAI(rsx, vx, 57600, 40960);
    SB;
    asm volatile("s_waitcnt vmcnt(7) lgkmcnt(0)\n\ts_barrier" ::: "memory");
    bf16x8 xf0 = RD128((LAS char*)(xb0) + 0);
    bf16x8 bs0 = RD128((LAS char*)(bb0) + 0);
    bf16x4 wag0_0a = TRR((LAS char*)(wb) + 0);
    bf16x4 wag0_0b = TRR((LAS char*)(wb) + 512); bf16x8 wag0_0 = CAT(wag0_0a,wag0_0b);
    bf16x4 wau0_0a = TRR((LAS char*)(wb) + 8192);
    bf16x4 wau0_0b = TRR((LAS char*)(wb) + 8704); bf16x8 wau0_0 = CAT(wau0_0a,wau0_0b);
    bf16x8 xf1 = RD128((LAS char*)(xb1) + 0);
    bf16x8 bs1 = RD128((LAS char*)(bb1) + 0);
    bf16x4 wag0_1a = TRR((LAS char*)(wb) + 2048);
    bf16x4 wag0_1b = TRR((LAS char*)(wb) + 2560); bf16x8 wag0_1 = CAT(wag0_1a,wag0_1b);
    SB;
    sacc = MFMA(xf0, bs0, zero16); PIN1(sacc);
    bf16x4 wau0_1a = TRR((LAS char*)(wb) + 10240);
    bf16x4 wau0_1b = TRR((LAS char*)(wb) + 10752); bf16x8 wau0_1 = CAT(wau0_1a,wau0_1b);
    DMAI(rsb, vb, 256, 81920);
    SB;
    ag0 = MFMA(wag0_0, xf0, zero16); PIN1(ag0);
    bf16x8 xf2 = RD128((LAS char*)(xb2) + 0);
    bf16x8 bs2 = RD128((LAS char*)(bb2) + 0);
    DMAI(rsx, vx, 384, 49152);
    float L2r = fmaf(Lr,Lr, -(Li*Li)); float L2i = (Lr+Lr)*Li;
    PIN2(L2r,L2i);
    SB;
    au0 = MFMA(wau0_0, xf0, zero16); PIN1(au0);
    bf16x4 wag0_2a = TRR((LAS char*)(wb) + 4096);
    bf16x4 wag0_2b = TRR((LAS char*)(wb) + 4608); bf16x8 wag0_2 = CAT(wag0_2a,wag0_2b);
    DMAI(rsx, vx, 57728, 57344);
    float L4r = fmaf(L2r,L2r, -(L2i*L2i)); float L4i = (L2r+L2r)*L2i;
    PIN2(L4r,L4i);
    SB;
    sacc = MFMA(xf1, bs1, sacc); PIN1(sacc);
    bf16x4 wau0_2a = TRR((LAS char*)(wb) + 12288);
    bf16x4 wau0_2b = TRR((LAS char*)(wb) + 12800); bf16x8 wau0_2 = CAT(wau0_2a,wau0_2b);
    DMAI(rsb, vb, 384, 90112);
    SB;
    ag0 = MFMA(wag0_1, xf1, ag0); PIN1(ag0);
    bf16x8 xf3 = RD128((LAS char*)(xb3) + 0);
    bf16x8 bs3 = RD128((LAS char*)(bb3) + 0);
    DMAI(rsx, vx, 512, 106496);
    float L8r = fmaf(L4r,L4r, -(L4i*L4i)); float L8i = (L4r+L4r)*L4i;
    PIN2(L8r,L8i);
    SB;
    au0 = MFMA(wau0_1, xf1, au0); PIN1(au0);
    bf16x4 wag0_3a = TRR((LAS char*)(wb) + 6144);
    bf16x4 wag0_3b = TRR((LAS char*)(wb) + 6656); bf16x8 wag0_3 = CAT(wag0_3a,wag0_3b);
    DMAI(rsx, vx, 57856, 114688);
    SB;
    sacc = MFMA(xf2, bs2, sacc); PIN1(sacc);
    bf16x4 wau0_3a = TRR((LAS char*)(wb) + 14336);
    bf16x4 wau0_3b = TRR((LAS char*)(wb) + 14848); bf16x8 wau0_3 = CAT(wau0_3a,wau0_3b);
    DMAI(rsb, vb, 512, 98304);
    float L16r = fmaf(L8r,L8r, -(L8i*L8i)); float L16i = (L8r+L8r)*L8i;
    PIN2(L16r,L16i);
    SB;
    ag0 = MFMA(wag0_2, xf2, ag0); PIN1(ag0);
    SB;
    asm volatile("s_waitcnt vmcnt(9) lgkmcnt(0)\n\ts_barrier" ::: "memory");
    bf16x8 xf4 = RD128((LAS char*)(xb0) + 16384);
    bf16x8 bs4 = RD128((LAS char*)(bb0) + 8192);
    DMAI(rsx, vx, 640, 122880);
    float L32r = fmaf(L16r,L16r, -(L16i*L16i)); float L32i = (L16r+L16r)*L16i;
    PIN2(L32r,L32i);
    SB;
    au0 = MFMA(wau0_2, xf2, au0); PIN1(au0);
    bf16x8 xf5 = RD128((LAS char*)(xb1) + 16384);
    bf16x8 bs5 = RD128((LAS char*)(bb1) + 8192);
    DMAI(rsx, vx, 57984, 131072);
    SB;
    sacc = MFMA(xf3, bs3, sacc); PIN1(sacc);
    bf16x8 xf6 = RD128((LAS char*)(xb2) + 16384);
    bf16x8 bs6 = RD128((LAS char*)(bb2) + 8192);
    DMAI(rsb, vb, 640, 65536);
    float L64r = fmaf(L32r,L32r, -(L32i*L32i)); float L64i = (L32r+L32r)*L32i;
    PIN2(L64r,L64i);
    SB;
    ag0 = MFMA(wag0_3, xf3, ag0); PIN1(ag0);
    bf16x8 xf7 = RD128((LAS char*)(xb3) + 16384);
    bf16x8 bs7 = RD128((LAS char*)(bb3) + 8192);
    DMAI(rsx, vx, 768, 0);
    SB;
    au0 = MFMA(wau0_3, xf3, au0); PIN1(au0);
    SB;
    asm volatile("s_waitcnt vmcnt(10) lgkmcnt(0)\n\ts_barrier" ::: "memory");
    bf16x8 xf8 = RD128((LAS char*)(xb0) + 32768);
    bf16x8 bs8 = RD128((LAS char*)(bb0) + 16384);
    DMAI(rsx, vx, 58112, 8192);
    float L128r = fmaf(L64r,L64r, -(L64i*L64i)); float L128i = (L64r+L64r)*L64i;
    PIN2(L128r,L128i);
    SB;
    sacc = MFMA(xf4, bs4, sacc); PIN1(sacc);
    bf16x8 xf9 = RD128((LAS char*)(xb1) + 32768);
    bf16x8 bs9 = RD128((LAS char*)(bb1) + 16384);
    DMAI(rsb, vb, 768, 73728);
    SB;
    sacc = MFMA(xf5, bs5, sacc); PIN1(sacc);
    bf16x8 xf10 = RD128((LAS char*)(xb2) + 32768);
    bf16x8 bs10 = RD128((LAS char*)(bb2) + 16384);
    DMAI(rsw1, vw, 229376, 16384);
    float f0r = (q&1) ? L8r : 1.f; float f0i = (q&1) ? L8i : 0.f;
    PIN2(f0r,f0i);
    SB;
    sacc = MFMA(xf6, bs6, sacc); PIN1(sacc);
    bf16x8 xf11 = RD128((LAS char*)(xb3) + 32768);
    bf16x8 bs11 = RD128((LAS char*)(bb3) + 16384);
    DMAI(rsw3, vw, 229376, 24576);
    float f1r = (q&2) ? L16r : 1.f; float f1i = (q&2) ? L16i : 0.f;
    PIN2(f1r,f1i);
    float Lqr = fmaf(f0r,f1r, -(f0i*f1i)); float Lqi = fmaf(f0r,f1i, f0i*f1r);
    PIN2(Lqr,Lqi);
    SB;
    sacc = MFMA(xf7, bs7, sacc); PIN1(sacc);
    SB;
    asm volatile("s_waitcnt vmcnt(11) lgkmcnt(0)\n\ts_barrier" ::: "memory");
    bf16x8 xf12 = RD128((LAS char*)(xb0) + 49152);
    bf16x8 bs12 = RD128((LAS char*)(bb0) + 24576);
    DMAI(rsw1, vw, 458752, 32768);
    SB;
    sacc = MFMA(xf8, bs8, sacc); PIN1(sacc);
    bf16x8 xf13 = RD128((LAS char*)(xb1) + 49152);
    bf16x8 bs13 = RD128((LAS char*)(bb1) + 24576);
    DMAI(rsw3, vw, 458752, 40960);
    float M1r = (q&2) ? f0r : 0.f, M1i = (q&2) ? f0i : 0.f;
    PIN2(M1r,M1i);
    SB;
    sacc = MFMA(xf9, bs9, sacc); PIN1(sacc);
    bf16x8 xf14 = RD128((LAS char*)(xb2) + 49152);
    bf16x8 bs14 = RD128((LAS char*)(bb2) + 24576);
    DMAI(rsw3, vw, 1146880, 81920);
    float f2r = (mh) ? L32r : 1.f; float f2i = (mh) ? L32i : 0.f;
    PIN2(f2r,f2i);
    SB;
    sacc = MFMA(xf10, bs10, sacc); PIN1(sacc);
    bf16x8 xf15 = RD128((LAS char*)(xb3) + 49152);
    bf16x8 bs15 = RD128((LAS char*)(bb3) + 24576);
    float f3r = fmaf(Lqr,f2r, -(Lqi*f2i)); float f3i = fmaf(Lqr,f2i, Lqi*f2r);
    PIN2(f3r,f3i);
    SB;
    sacc = MFMA(xf11, bs11, sacc); PIN1(sacc);
    SB;
    asm volatile("s_waitcnt vmcnt(11) lgkmcnt(0)\n\ts_barrier" ::: "memory");
    xb0 += 106496; PIN1(xb0);
    bf16x8 xf16 = RD128((LAS char*)(xb0) + 0);
    bf16x8 bs16 = RD128((LAS char*)(bb0) + 32768);
    DMAI(rsw1, vw, 1376256, 139264);
    SB;
    sacc = MFMA(xf12, bs12, sacc); PIN1(sacc);
    xb1 += 106496; PIN1(xb1);
    bf16x8 xf17 = RD128((LAS char*)(xb1) + 0);
    bf16x8 bs17 = RD128((LAS char*)(bb1) + 32768);
    DMAI(rsw3, vw, 1376256, 147456);
    float f4r = (mu) ? L64r : 1.f; float f4i = (mu) ? L64i : 0.f;
    PIN2(f4r,f4i);
    float LLr = fmaf(f3r,f4r, -(f3i*f4i)); float LLi = fmaf(f3r,f4i, f3i*f4r);
    PIN2(LLr,LLi);
    SB;
    sacc = MFMA(xf13, bs13, sacc); PIN1(sacc);
    xb2 += 106496; PIN1(xb2);
    bf16x8 xf18 = RD128((LAS char*)(xb2) + 0);
    bf16x8 bs18 = RD128((LAS char*)(bb2) + 32768);
    SB;
    sacc = MFMA(xf14, bs14, sacc); PIN1(sacc);
    xb3 += 106496; PIN1(xb3);
    bf16x8 xf19 = RD128((LAS char*)(xb3) + 0);
    bf16x8 bs19 = RD128((LAS char*)(bb3) + 32768);
    DMAI(rsw1, vw, 1792, 90112);
    float L256r = fmaf(L128r,L128r, -(L128i*L128i)); float L256i = (L128r+L128r)*L128i;
    PIN2(L256r,L256i);
    SB;
    sacc = MFMA(xf15, bs15, sacc); PIN1(sacc);
    SB;
    asm volatile("s_waitcnt vmcnt(11) lgkmcnt(0)\n\ts_barrier" ::: "memory");
    bf16x8 xf20 = RD128((LAS char*)(xb0) + 16384);
    bf16x8 bs20 = RD128((LAS char*)(bb0) + 0);
    SB;
    sacc = MFMA(xf16, bs16, sacc); PIN1(sacc);
    bf16x8 xf21 = RD128((LAS char*)(xb1) + 16384);
    bf16x8 bs21 = RD128((LAS char*)(bb1) + 0);
    DMAI(rsw1, vw, 688128, 106496);
    float L512r = fmaf(L256r,L256r, -(L256i*L256i)); float L512i = (L256r+L256r)*L256i;
    PIN2(L512r,L512i);
    SB;
    sacc = MFMA(xf17, bs17, sacc); PIN1(sacc);
    bf16x8 xf22 = RD128((LAS char*)(xb2) + 16384);
    bf16x8 bs22 = RD128((LAS char*)(bb2) + 0);
    DMAI(rsw3, vw, 688128, 114688);
    u32x4 rsc = (wd >> 2) ? rsci : rscr; int vc = (64 * ct + 16 * (wd & 3) + (lane >> 2)) * 896 + 448 * rh + 16 * ((lane & 3) ^ q); asm volatile("" : "+s"(rsc), "+v"(vc));
    SB;
    sacc = MFMA(xf18, bs18, sacc); PIN1(sacc);
    bf16x8 xf23 = RD128((LAS char*)(xb3) + 16384);
    bf16x8 bs23 = RD128((LAS char*)(bb3) + 0);
    float L1024r = fmaf(L512r,L512r, -(L512i*L512i)); float L1024i = (L512r+L512r)*L512i;
    PIN2(L1024r,L1024i);
    SB;
    sacc = MFMA(xf19, bs19, sacc); PIN1(sacc);
    SB;
    asm volatile("s_waitcnt vmcnt(10) lgkmcnt(0)\n\ts_barrier" ::: "memory");
    xb0 += -106496; PIN1(xb0);
    bf16x8 xf24 = RD128((LAS char*)(xb0) + 0);
    bf16x8 bs24 = RD128((LAS char*)(bb0) + 8192);
    DMAI(rsw1, vw, 917504, 122880);
    const int exq = ex < 0 ? 0 : ex;
    SB;
    sacc = MFMA(xf20, bs20, sacc); PIN1(sacc);
    xb1 += -106496; PIN1(xb1);
    bf16x8 xf25 = RD128((LAS char*)(xb1) + 0);
    bf16x8 bs25 = RD128((LAS char*)(bb1) + 8192);
    DMAI(rsw3, vw, 917504, 131072);
    float g0r = (exq&1) ? L128r : 1.f; float g0i = (exq&1) ? L128i : 0.f;
    PIN2(g0r,g0i);
    SB;
    sacc = MFMA(xf21, bs21, sacc); PIN1(sacc);
    xb2 += -106496; PIN1(xb2);
    bf16x8 xf26 = RD128((LAS char*)(xb2) + 0);
    bf16x8 bs26 = RD128((LAS char*)(bb2) + 8192);
    float g1r = (exq&2) ? L256r : 1.f; float g1i = (exq&2) ? L256i : 0.f;
    PIN2(g1r,g1i);
    SB;
    sacc = MFMA(xf22, bs22, sacc); PIN1(sacc);
    xb3 += -106496; PIN1(xb3);
    bf16x8 xf27 = RD128((LAS char*)(xb3) + 0);
    bf16x8 bs27 = RD128((LAS char*)(bb3) + 8192);
    DMAI(rsw1, vw, 231168, 98304);
    float g2r = fmaf(g0r,g1r, -(g0i*g1i)); float g2i = fmaf(g0r,g1i, g0i*g1r);
    PIN2(g2r,g2i);
    SB;
    sacc = MFMA(xf23, bs23, sacc); PIN1(sacc);
    SB;
    asm volatile("s_waitcnt vmcnt(11) lgkmcnt(0)\n\ts_barrier" ::: "memory");
    wb += -122880; PIN1(wb);
    bf16x4 wag0_4a = TRR((LAS char*)(wb) + 0);
    bf16x4 wag0_4b = TRR((LAS char*)(wb) + 512); bf16x8 wag0_4 = CAT(wag0_4a,wag0_4b);
    float g3r = (exq&4) ? L512r : 1.f; float g3i = (exq&4) ? L512i : 0.f;
    PIN2(g3r,g3i);
    SB;
    sacc = MFMA(xf24, bs24, sacc); PIN1(sacc);
    bf16x4 wau0_4a = TRR((LAS char*)(wb) + 8192);
    bf16x4 wau0_4b = TRR((LAS char*)(wb) + 8704); bf16x8 wau0_4 = CAT(wau0_4a,wau0_4b);
    DMAI(rsw1, vw, 1146880, 73728);
    float g4r = fmaf(g2r,g3r, -(g2i*g3i)); float g4i = fmaf(g2r,g3i, g2i*g3r);
    PIN2(g4r,g4i);
    SB;
    sacc = MFMA(xf25, bs25, sacc); PIN1(sacc);
    bf16x4 wag0_5a = TRR((LAS char*)(wb) + 2048);
    bf16x4 wag0_5b = TRR((LAS char*)(wb) + 2560); bf16x8 wag0_5 = CAT(wag0_5a,wag0_5b);
    int vsg = (q * 448 + st) * 16, vgg = perm32(i32) * 16; unsigned esp = sb + LM + (ni * 16 + st) * 32; asm volatile("" : "+v"(vsg), "+v"(vgg), "+v"(esp));
    SB;
    sacc = MFMA(xf26, bs26, sacc); PIN1(sacc);
    bf16x4 wau0_5a = TRR((LAS char*)(wb) + 10240);
    bf16x4 wau0_5b = TRR((LAS char*)(wb) + 10752); bf16x8 wau0_5 = CAT(wau0_5a,wau0_5b);
    float g5r = (exq&8) ? L1024r : 1.f; float g5i = (exq&8) ? L1024i : 0.f;
    PIN2(g5r,g5i);
    SB;
    sacc = MFMA(xf27, bs27, sacc); PIN1(sacc);
    bf16x4 wag0_6a = TRR((LAS char*)(wb) + 4096);
    bf16x4 wag0_6b = TRR((LAS char*)(wb) + 4608); bf16x8 wag0_6 = CAT(wag0_6a,wag0_6b);
    float Pwr = fmaf(g4r,g5r, -(g4i*g5i)); float Pwi = fmaf(g4r,g5i, g4i*g5r);
    PIN2(Pwr,Pwi);
    SB;
    ag0 = MFMA(wag0_4, xf4, ag0); PIN1(ag0);
    SB;
    bf16x4 wau0_6a = TRR((LAS char*)(wb) + 12288);
    bf16x4 wau0_6b = TRR((LAS char*)(wb) + 12800); bf16x8 wau0_6 = CAT(wau0_6a,wau0_6b);
    SB;
    au0 = MFMA(wau0_4, xf4, au0); PIN1(au0);
    SB;
    bf16x4 wag0_7a = TRR((LAS char*)(wb) + 6144);
    bf16x4 wag0_7b = TRR((LAS char*)(wb) + 6656); bf16x8 wag0_7 = CAT(wag0_7a,wag0_7b);
    SWAP8(sacc);
    float e1r = fmaf(Lr,v0r, fmaf(-Li,v0i,v1r)); float e1i = fmaf(Lr,v0i, fmaf(Li,v0r,v1i));
    PIN2(e1r,e1i);
    float e2r = fmaf(Lr,e1r, fmaf(-Li,e1i,v2r)); float e2i = fmaf(Lr,e1i, fmaf(Li,e1r,v2i));
    PIN2(e2r,e2i);
    float e3r = fmaf(Lr,e2r, fmaf(-Li,e2i,v3r)); float e3i = fmaf(Lr,e2i, fmaf(Li,e2r,v3i));
    PIN2(e3r,e3i);
    float e4r = fmaf(Lr,e3r, fmaf(-Li,e3i,v4r)); float e4i = fmaf(Lr,e3i, fmaf(Li,e3r,v4i));
    PIN2(e4r,e4i);
    float e5r = fmaf(Lr,e4r, fmaf(-Li,e4i,v5r)); float e5i = fmaf(Lr,e4i, fmaf(Li,e4r,v5i));
    PIN2(e5r,e5i);
    SB;
    ag0 = MFMA(wag0_5, xf5, ag0); PIN1(ag0);
    bf16x4 wau0_7a = TRR((LAS char*)(wb) + 14336);
    bf16x4 wau0_7b = TRR((LAS char*)(wb) + 14848); bf16x8 wau0_7 = CAT(wau0_7a,wau0_7b);
    float e6r = fmaf(Lr,e5r, fmaf(-Li,e5i,v6r)); float e6i = fmaf(Lr,e5i, fmaf(Li,e5r,v6i));
    PIN2(e6r,e6i);
    float e7r = fmaf(Lr,e6r, fmaf(-Li,e6i,v7r)); float e7i = fmaf(Lr,e6i, fmaf(Li,e6r,v7i));
    PIN2(e7r,e7i);
    float a1r, b1r, a1i, b1i; SWAP16(e7r, e7r, a1r, b1r); SWAP16(e7i, e7i, a1i, b1i);
    PIN4(a1r,b1r,a1i,b1i);
    float T1r = fmaf(L8r,a1r, fmaf(-L8i,a1i,b1r)); float T1i = fmaf(L8r,a1i, fmaf(L8i,a1r,b1i));
    PIN2(T1r,T1i);
    float c1r, d1r, c1i, d1i; SWAP32(T1r, T1r, c1r, d1r); SWAP32(T1i, T1i, c1i, d1i);
    PIN4(c1r,d1r,c1i,d1i);
    float W1r = fmaf(L16r,c1r, fmaf(-L16i,c1i,d1r)); float W1i = fmaf(L16r,c1i, fmaf(L16i,c1r,d1i));
    PIN2(W1r,W1i);
    *(LAS f32x2*)((LAS char*)(esp) + mi*8) = f32x2{W1r, W1i};
    SB;
    au0 = MFMA(wau0_5, xf5, au0); PIN1(au0);
    SB;
    asm volatile("s_waitcnt vmcnt(10) lgkmcnt(0)\n\ts_barrier" ::: "memory");
    bf16x4 wag0_8a = TRR((LAS char*)(wb) + 16384);
    bf16x4 wag0_8b = TRR((LAS char*)(wb) + 16896); bf16x8 wag0_8 = CAT(wag0_8a,wag0_8b);
    f32x4 wq0 = *(LAS f32x4*)(LAS char*)(esp), wq1 = *(LAS f32x4*)((LAS char*)(esp) + 16);
    a1r = (q&1) ? a1r : 0.f; a1i = (q&1) ? a1i : 0.f;
    PIN2(a1r,a1i);
    float P1r = fmaf(M1r,c1r, fmaf(-M1i,c1i,a1r)); float P1i = fmaf(M1r,c1i, fmaf(M1i,c1r,a1i));
    PIN2(P1r,P1i);
    SB;
    ag0 = MFMA(wag0_6, xf6, ag0); PIN1(ag0);
    bf16x4 wau0_8a = TRR((LAS char*)(wb) + 24576);
    bf16x4 wau0_8b = TRR((LAS char*)(wb) + 25088); bf16x8 wau0_8 = CAT(wau0_8a,wau0_8b);
    DMAK(rsc, vc, "s_lshr_b32", kq0, 2, 0);
    float Q1r = wq0[0], Q1i = wq0[1], X1r = wq0[2], X1i = wq0[3], X2r = wq1[0], X2i = wq1[1], X3r = wq1[2], X3i = wq1[3];
    float Q2r = fmaf(L32r,Q1r, fmaf(-L32i,Q1i,X1r)); float Q2i = fmaf(L32r,Q1i, fmaf(L32i,Q1r,X1i));
    PIN2(Q2r,Q2i);
    float Q3r = fmaf(L32r,Q2r, fmaf(-L32i,Q2i,X2r)); float Q3i = fmaf(L32r,Q2i, fmaf(L32i,Q2r,X2i));
    PIN2(Q3r,Q3i);
    float Sr = fmaf(L32r,Q3r, fmaf(-L32i,Q3i,X3r)); float Si = fmaf(L32r,Q3i, fmaf(L32i,Q3r,X3i));
    PIN2(Sr,Si);
    SB;
    au0 = MFMA(wau0_6, xf6, au0); PIN1(au0);
    bf16x4 wag0_9a = TRR((LAS char*)(wb) + 18432);
    bf16x4 wag0_9b = TRR((LAS char*)(wb) + 18944); bf16x8 wag0_9 = CAT(wag0_9a,wag0_9b);
    if (mi == 0 && q == 0) { u32x4 sg = {__float_as_uint(Sr), tagl, __float_as_uint(Si), tagh}; ST128(sg, vsg, sgo); }
    float Qr = mi==0 ? 0.f : mi==1 ? Q1r : mi==2 ? Q2r : Q3r; float Qi = mi==0 ? 0.f : mi==1 ? Q1i : mi==2 ? Q2i : Q3i;
    PIN2(Qr,Qi);
    SB;
    ag0 = MFMA(wag0_7, xf7, ag0); PIN1(ag0);
    bf16x4 wau0_9a = TRR((LAS char*)(wb) + 26624);
    bf16x4 wau0_9b = TRR((LAS char*)(wb) + 27136); bf16x8 wau0_9 = CAT(wau0_9a,wau0_9b);
    DMAK(rsc, vc, "s_lshr_b32", kq1, 2, 8192);
    float P2r = fmaf(Lqr,Qr, fmaf(-Lqi,Qi,P1r)); float P2i = fmaf(Lqr,Qi, fmaf(Lqi,Qr,P1i));
    PIN2(P2r,P2i);
    SB;
    au0 = MFMA(wau0_7, xf7, au0); PIN1(au0);
    bf16x4 wag0_10a = TRR((LAS char*)(wb) + 20480);
    bf16x4 wag0_10b = TRR((LAS char*)(wb) + 20992); bf16x8 wag0_10 = CAT(wag0_10a,wag0_10b);
    int vsi = ex < 0 ? OOB : vsg; asm volatile("" : "+v"(vsi));
    SB;
    ag0 = MFMA(wag0_8, xf8, ag0); PIN1(ag0);
    bf16x4 wau0_10a = TRR((LAS char*)(wb) + 28672);
    bf16x4 wau0_10b = TRR((LAS char*)(wb) + 29184); bf16x8 wau0_10 = CAT(wau0_10a,wau0_10b);
    u32x4 gv; LD128(gv, vgg, ggo);
    DMAK(rsc, vc, "s_lshr_b32", kq2, 2, 16384);
    SB;
    au0 = MFMA(wau0_8, xf8, au0); PIN1(au0);
    bf16x4 wag0_11a = TRR((LAS char*)(wb) + 22528);
    bf16x4 wag0_11b = TRR((LAS char*)(wb) + 23040); bf16x8 wag0_11 = CAT(wag0_11a,wag0_11b);
    unsigned gsw = sb + LM + 2048 + 128 * mi + (vgg >> 2); asm volatile("" : "+v"(gsw));
    SB;
    ag0 = MFMA(wag0_9, xf9, ag0); PIN1(ag0);
    bf16x4 wau0_11a = TRR((LAS char*)(wb) + 30720);
    bf16x4 wau0_11b = TRR((LAS char*)(wb) + 31232); bf16x8 wau0_11 = CAT(wau0_11a,wau0_11b);
    DMAK(rsc, vc, "s_lshr_b32", kq3, 2, 24576);
    SB;
    au0 = MFMA(wau0_9, xf9, au0); PIN1(au0);
    SB;
    asm volatile("s_waitcnt vmcnt(9) lgkmcnt(0)\n\ts_barrier" ::: "memory");
    wb += 57344; PIN1(wb);
    bf16x4 wag0_12a = TRR((LAS char*)(wb) + 32768);
    bf16x4 wag0_12b = TRR((LAS char*)(wb) + 33280); bf16x8 wag0_12 = CAT(wag0_12a,wag0_12b);
    SB;
    ag0 = MFMA(wag0_10, xf10, ag0); PIN1(ag0);
    bf16x4 wau0_12a = TRR((LAS char*)(wb) + 40960);
    bf16x4 wau0_12b = TRR((LAS char*)(wb) + 41472); bf16x8 wau0_12 = CAT(wau0_12a,wau0_12b);
    DMAK(rsc, vc, "s_lshr_b32", kq4, 2, 32768);
    SB;
    au0 = MFMA(wau0_10, xf10, au0); PIN1(au0);
    bf16x4 wag0_13a = TRR((LAS char*)(wb) + 34816);
    bf16x4 wag0_13b = TRR((LAS char*)(wb) + 35328); bf16x8 wag0_13 = CAT(wag0_13a,wag0_13b);
    int v2 = (448 * rh + 8 * wd + l3) * 896 + 128 * ct + fw; asm volatile("" : "+v"(v2));
    SB;
    ag0 = MFMA(wag0_11, xf11, ag0); PIN1(ag0);
    bf16x4 wau0_13a = TRR((LAS char*)(wb) + 43008);
    bf16x4 wau0_13b = TRR((LAS char*)(wb) + 43520); bf16x8 wau0_13 = CAT(wau0_13a,wau0_13b);
    DMAK(rsc, vc, "s_lshr_b32", kq5, 2, 40960);
    SB;
    au0 = MFMA(wau0_11, xf11, au0); PIN1(au0);
    bf16x4 wag0_14a = TRR((LAS char*)(wb) + 36864);
    bf16x4 wag0_14b = TRR((LAS char*)(wb) + 37376); bf16x8 wag0_14 = CAT(wag0_14a,wag0_14b);
    int l16 = lane * 16; unsigned gsr = sb + LM + 2048 + (32 * mi + 8 * q) * 4; asm volatile("" : "+v"(l16), "+v"(gsr));
    SB;
    ag0 = MFMA(wag0_12, xf12, ag0); PIN1(ag0);
    bf16x4 wau0_14a = TRR((LAS char*)(wb) + 45056);
    bf16x4 wau0_14b = TRR((LAS char*)(wb) + 45568); bf16x8 wau0_14 = CAT(wau0_14a,wau0_14b);
    u32x4 sgv; LD128(sgv, vsi, sgi);
    SB;
    au0 = MFMA(wau0_12, xf12, au0); PIN1(au0);
    bf16x4 wag0_15a = TRR((LAS char*)(wb) + 38912);
    bf16x4 wag0_15b = TRR((LAS char*)(wb) + 39424); bf16x8 wag0_15 = CAT(wag0_15a,wag0_15b);
    SB;
    ag0 = MFMA(wag0_13, xf13, ag0); PIN1(ag0);
    bf16x4 wau0_15a = TRR((LAS char*)(wb) + 47104);
    bf16x4 wau0_15b = TRR((LAS char*)(wb) + 47616); bf16x8 wau0_15 = CAT(wau0_15a,wau0_15b);
    DMAK(rsc, vc, "s_lshr_b32", kq6, 2, 49152);
    SB;
    au0 = MFMA(wau0_13, xf13, au0); PIN1(au0);
    SB;
    asm volatile("s_waitcnt vmcnt(11) lgkmcnt(0)\n\ts_barrier" ::: "memory");
    bf16x4 wag0_16a = TRR((LAS char*)(wb) + 49152);
    bf16x4 wag0_16b = TRR((LAS char*)(wb) + 49664); bf16x8 wag0_16 = CAT(wag0_16a,wag0_16b);
    SB;
    ag0 = MFMA(wag0_14, xf14, ag0); PIN1(ag0);
    bf16x4 wau0_16a = TRR((LAS char*)(wb) + 57344);
    bf16x4 wau0_16b = TRR((LAS char*)(wb) + 57856); bf16x8 wau0_16 = CAT(wau0_16a,wau0_16b);
    DMAI(rsw1, vw, 460544, 106496);
    asm volatile("s_waitcnt vmcnt(7)" : "+v"(gv));
    if (__builtin_expect(ANY(gv[1]!=tagl || gv[3]!=tagh), 0)) { for (int sp = 0; sp < SPINMAX; ++sp) { __builtin_amdgcn_s_sleep(1); LD128(gv, vgg, ggo); asm volatile("s_waitcnt vmcnt(0)" : "+v"(gv)); if (!(ANY(gv[1]!=tagl || gv[3]!=tagh))) break; } }
    float cg = __uint_as_float(gv[2]); *(LAS float*)(LAS char*)(gsw) = __uint_as_float(gv[0]);
    PIN1(cg);
    SB;
    au0 = MFMA(wau0_14, xf14, au0); PIN1(au0);
    bf16x4 wag0_17a = TRR((LAS char*)(wb) + 51200);
    bf16x4 wag0_17b = TRR((LAS char*)(wb) + 51712); bf16x8 wag0_17 = CAT(wag0_17a,wag0_17b);
    int tk0 = 64 * rh + 32 * mu + 4 * (w & 3) + q + 2 * (st & 8), vde = 128 * ct + 8 * st, vxe = (128 * m + tk0) * 896 + vde; int vxf = vxe + (st & 8 ? -14336 : 14336); asm volatile("" : "+v"(vde), "+v"(vxe), "+v"(vxf));
    SB;
    ag0 = MFMA(wag0_15, xf15, ag0); PIN1(ag0);
    bf16x4 wau0_17a = TRR((LAS char*)(wb) + 59392);
    bf16x4 wau0_17b = TRR((LAS char*)(wb) + 59904); bf16x8 wau0_17 = CAT(wau0_17a,wau0_17b);
    DMAI(rsw1, vw, 689920, 114688);
    SB;
    au0 = MFMA(wau0_15, xf15, au0); PIN1(au0);
    bf16x4 wag0_18a = TRR((LAS char*)(wb) + 53248);
    bf16x4 wag0_18b = TRR((LAS char*)(wb) + 53760); bf16x8 wag0_18 = CAT(wag0_18a,wag0_18b);
    SB;
    ag0 = MFMA(wag0_16, xf16, ag0); PIN1(ag0);
    bf16x4 wau0_18a = TRR((LAS char*)(wb) + 61440);
    bf16x4 wau0_18b = TRR((LAS char*)(wb) + 61952); bf16x8 wau0_18 = CAT(wau0_18a,wau0_18b);
    DMAK(rsw2, v2, "s_mul_i32", kq0, 224, 57344);
    asm volatile("s_waitcnt vmcnt(4)" : "+v"(sgv));
    if (__builtin_expect(ANY((sgv[1]!=tagl || sgv[3]!=tagh) && ex >= 0), 0)) { for (int sp = 0; sp < SPINMAX; ++sp) { __builtin_amdgcn_s_sleep(1); LD128(sgv, vsi, sgi); asm volatile("s_waitcnt vmcnt(0)" : "+v"(sgv)); if (!(ANY((sgv[1]!=tagl || sgv[3]!=tagh) && ex >= 0))) break; } }
    float Gr = __uint_as_float(sgv[0]), Gi = __uint_as_float(sgv[2]);
    float Ur = fmaf(Pwr,Gr, -(Pwi*Gi)); float Ui = fmaf(Pwr,Gi, Pwi*Gr);
    PIN2(Ur,Ui);
    SB;
    au0 = MFMA(wau0_16, xf16, au0); PIN1(au0);
    bf16x4 wag0_19a = TRR((LAS char*)(wb) + 55296);
    bf16x4 wag0_19b = TRR((LAS char*)(wb) + 55808); bf16x8 wag0_19 = CAT(wag0_19a,wag0_19b);
    float ua, ub; SWAP16(Ur, Ui, ua, ub); float ut = ua+ub;
    PIN1(ut);
    SB;
    ag0 = MFMA(wag0_17, xf17, ag0); PIN1(ag0);
    bf16x4 wau0_19a = TRR((LAS char*)(wb) + 63488);
    bf16x4 wau0_19b = TRR((LAS char*)(wb) + 64000); bf16x8 wau0_19 = CAT(wau0_19a,wau0_19b);
    const u32x4 sent = {~0u, ~0u, ~0u, ~0u};
    STX(sent, pli, 0);
    SWAP32(ut, ut, ua, ub); ut = ua+ub;
    PIN1(ut);
    *(LAS float*)((LAS char*)(esp) + 1024 + mi*8 + (q&1)*4) = ut;
    SB;
    au0 = MFMA(wau0_17, xf17, au0); PIN1(au0);
    SB;
    asm volatile("s_waitcnt vmcnt(13) lgkmcnt(0)\n\ts_barrier" ::: "memory");
    bf16x4 wag0_20a = TRR((LAS char*)(wb) + 0);
    bf16x4 wag0_20b = TRR((LAS char*)(wb) + 512); bf16x8 wag0_20 = CAT(wag0_20a,wag0_20b);
    f32x4 cq0 = *(LAS f32x4*)((LAS char*)(esp) + 1024), cq1 = *(LAS f32x4*)((LAS char*)(esp) + 1024 + 16); f32x4 gq0 = *(LAS f32x4*)(LAS char*)(gsr), gq1 = *(LAS f32x4*)((LAS char*)(gsr) + 16);
    SB;
    ag0 = MFMA(wag0_18, xf18, ag0); PIN1(ag0);
    bf16x4 wau0_20a = TRR((LAS char*)(wb) + 8192);
    bf16x4 wau0_20b = TRR((LAS char*)(wb) + 8704); bf16x8 wau0_20 = CAT(wau0_20a,wau0_20b);
    STX(sent, pli, 1);
    float Yr = (cq0[0]+cq0[2])+(cq1[0]+cq1[2]), Yi = (cq0[1]+cq0[3])+(cq1[1]+cq1[3]);
    PIN2(Yr,Yi);
    SB;
    au0 = MFMA(wau0_18, xf18, au0); PIN1(au0);
    bf16x4 wag0_21a = TRR((LAS char*)(wb) + 2048);
    bf16x4 wag0_21b = TRR((LAS char*)(wb) + 2560); bf16x8 wag0_21 = CAT(wag0_21a,wag0_21b);
    float Ir = fmaf(LLr,Yr, fmaf(-LLi,Yi,P2r)); float Ii = fmaf(LLr,Yi, fmaf(LLi,Yr,P2i));
    PIN2(Ir,Ii);
    float s0r = fmaf(Lr,Ir, fmaf(-Li,Ii,v0r)); float s0i = fmaf(Lr,Ii, fmaf(Li,Ir,v0i));
    PIN2(s0r,s0i);
    SB;
    ag0 = MFMA(wag0_19, xf19, ag0); PIN1(ag0);
    bf16x4 wau0_21a = TRR((LAS char*)(wb) + 10240);
    bf16x4 wau0_21b = TRR((LAS char*)(wb) + 10752); bf16x8 wau0_21 = CAT(wau0_21a,wau0_21b);
    STX(sent, hso + 458752, 0);
    float K0r = Kr*gq0[0], K0i = Ki*gq0[0];
    PIN2(K0r,K0i);
    float o0r = fmaf(K0r,s0r,-(K0i*s0i)); float o0i = fmaf(-K0r,s0i,-(K0i*s0r));
    PIN2(o0r,o0i);
    SB;
    au0 = MFMA(wau0_19, xf19, au0); PIN1(au0);
    bf16x4 wag0_22a = TRR((LAS char*)(wb) + 4096);
    bf16x4 wag0_22b = TRR((LAS char*)(wb) + 4608); bf16x8 wag0_22 = CAT(wag0_22a,wag0_22b);
    float s1r = fmaf(Lr,s0r, fmaf(-Li,s0i,v1r)); float s1i = fmaf(Lr,s0i, fmaf(Li,s0r,v1i));
    PIN2(s1r,s1i);
    SB;
    ag0 = MFMA(wag0_20, xf20, ag0); PIN1(ag0);
    bf16x4 wau0_22a = TRR((LAS char*)(wb) + 12288);
    bf16x4 wau0_22b = TRR((LAS char*)(wb) + 12800); bf16x8 wau0_22 = CAT(wau0_22a,wau0_22b);
    STX(sent, hso + 458752, 1);
    float K1r = Kr*gq0[1], K1i = Ki*gq0[1];
    PIN2(K1r,K1i);
    float o1r = fmaf(K1r,s1r,-(K1i*s1i)); float o1i = fmaf(-K1r,s1i,-(K1i*s1r));
    PIN2(o1r,o1i);
    SB;
    au0 = MFMA(wau0_20, xf20, au0); PIN1(au0);
    bf16x4 wag0_23a = TRR((LAS char*)(wb) + 6144);
    bf16x4 wag0_23b = TRR((LAS char*)(wb) + 6656); bf16x8 wag0_23 = CAT(wag0_23a,wag0_23b);
    unsigned pr0 = PK(o0r,o1r), pi0 = PK(o0i,o1i);
    PIN2(pr0,pi0);
    float s2r = fmaf(Lr,s1r, fmaf(-Li,s1i,v2r)); float s2i = fmaf(Lr,s1i, fmaf(Li,s1r,v2i));
    PIN2(s2r,s2i);
    SB;
    ag0 = MFMA(wag0_21, xf21, ag0); PIN1(ag0);
    bf16x4 wau0_23a = TRR((LAS char*)(wb) + 14336);
    bf16x4 wau0_23b = TRR((LAS char*)(wb) + 14848); bf16x8 wau0_23 = CAT(wau0_23a,wau0_23b);
    u32x2 xe0; LDX(xe0, rsx, vxe, 0);
    float K2r = Kr*gq0[2], K2i = Ki*gq0[2];
    PIN2(K2r,K2i);
    float o2r = fmaf(K2r,s2r,-(K2i*s2i)); float o2i = fmaf(-K2r,s2i,-(K2i*s2r));
    PIN2(o2r,o2i);
    SB;
    au0 = MFMA(wau0_21, xf21, au0); PIN1(au0);
    wb += 16384; PIN1(wb);
    bf16x4 wag0_24a = TRR((LAS char*)(wb) + 49152);
    bf16x4 wag0_24b = TRR((LAS char*)(wb) + 49664); bf16x8 wag0_24 = CAT(wag0_24a,wag0_24b);
    u32x2 xe1; LDX(xe1, rsx, vxf, 0);
    float s3r = fmaf(Lr,s2r, fmaf(-Li,s2i,v3r)); float s3i = fmaf(Lr,s2i, fmaf(Li,s2r,v3i));
    PIN2(s3r,s3i);
    SB;
    ag0 = MFMA(wag0_22, xf22, ag0); PIN1(ag0);
    bf16x4 wau0_24a = TRR((LAS char*)(wb) + 57344);
    bf16x4 wau0_24b = TRR((LAS char*)(wb) + 57856); bf16x8 wau0_24 = CAT(wau0_24a,wau0_24b);
    u32x2 de; LDX(de, rsd, vde, 0);
    float K3r = Kr*gq0[3], K3i = Ki*gq0[3];
    PIN2(K3r,K3i);
    float o3r = fmaf(K3r,s3r,-(K3i*s3i)); float o3i = fmaf(-K3r,s3i,-(K3i*s3r));
    PIN2(o3r,o3i);
    SB;
    au0 = MFMA(wau0_22, xf22, au0); PIN1(au0);
    bf16x4 wag0_25a = TRR((LAS char*)(wb) + 51200);
    bf16x4 wag0_25b = TRR((LAS char*)(wb) + 51712); bf16x8 wag0_25 = CAT(wag0_25a,wag0_25b);
    unsigned pr1 = PK(o2r,o3r), pi1 = PK(o2i,o3i);
    PIN2(pr1,pi1);
    float s4r = fmaf(Lr,s3r, fmaf(-Li,s3i,v4r)); float s4i = fmaf(Lr,s3i, fmaf(Li,s3r,v4i));
    PIN2(s4r,s4i);
    SB;
    ag0 = MFMA(wag0_23, xf23, ag0); PIN1(ag0);
    bf16x4 wau0_25a = TRR((LAS char*)(wb) + 59392);
    bf16x4 wau0_25b = TRR((LAS char*)(wb) + 59904); bf16x8 wau0_25 = CAT(wau0_25a,wau0_25b);
    float K4r = Kr*gq1[0], K4i = Ki*gq1[0];
    PIN2(K4r,K4i);
    float o4r = fmaf(K4r,s4r,-(K4i*s4i)); float o4i = fmaf(-K4r,s4i,-(K4i*s4r));
    PIN2(o4r,o4i);
    SB;
    au0 = MFMA(wau0_23, xf23, au0); PIN1(au0);
    bf16x4 wag0_26a = TRR((LAS char*)(wb) + 53248);
    bf16x4 wag0_26b = TRR((LAS char*)(wb) + 53760); bf16x8 wag0_26 = CAT(wag0_26a,wag0_26b);
    float s5r = fmaf(Lr,s4r, fmaf(-Li,s4i,v5r)); float s5i = fmaf(Lr,s4i, fmaf(Li,s4r,v5i));
    PIN2(s5r,s5i);
    SB;
    ag0 = MFMA(wag0_24, xf24, ag0); PIN1(ag0);
    bf16x4 wau0_26a = TRR((LAS char*)(wb) + 61440);
    bf16x4 wau0_26b = TRR((LAS char*)(wb) + 61952); bf16x8 wau0_26 = CAT(wau0_26a,wau0_26b);
    float K5r = Kr*gq1[1], K5i = Ki*gq1[1];
    PIN2(K5r,K5i);
    float o5r = fmaf(K5r,s5r,-(K5i*s5i)); float o5i = fmaf(-K5r,s5i,-(K5i*s5r));
    PIN2(o5r,o5i);
    SB;
    au0 = MFMA(wau0_24, xf24, au0); PIN1(au0);
    bf16x4 wag0_27a = TRR((LAS char*)(wb) + 55296);
    bf16x4 wag0_27b = TRR((LAS char*)(wb) + 55808); bf16x8 wag0_27 = CAT(wag0_27a,wag0_27b);
    unsigned pr2 = PK(o4r,o5r), pi2 = PK(o4i,o5i);
    PIN2(pr2,pi2);
    float s6r = fmaf(Lr,s5r, fmaf(-Li,s5i,v6r)); float s6i = fmaf(Lr,s5i, fmaf(Li,s5r,v6i));
    PIN2(s6r,s6i);
    SB;
    ag0 = MFMA(wag0_25, xf25, ag0); PIN1(ag0);
    bf16x4 wau0_27a = TRR((LAS char*)(wb) + 63488);
    bf16x4 wau0_27b = TRR((LAS char*)(wb) + 64000); bf16x8 wau0_27 = CAT(wau0_27a,wau0_27b);
    float K6r = Kr*gq1[2], K6i = Ki*gq1[2];
    PIN2(K6r,K6i);
    float o6r = fmaf(K6r,s6r,-(K6i*s6i)); float o6i = fmaf(-K6r,s6i,-(K6i*s6r));
    PIN2(o6r,o6i);
    SB;
    au0 = MFMA(wau0_25, xf25, au0); PIN1(au0);
    float s7r = fmaf(Lr,s6r, fmaf(-Li,s6i,v7r)); float s7i = fmaf(Lr,s6i, fmaf(Li,s6r,v7i));
    PIN2(s7r,s7i);
    unsigned tpw = sb + 65536 + w * 1024 + q * 256 + (st ^ (4 * q)) * 16, tpr = sb + 65536 + w * 1024 + (2 * (c4 & 1) + b16) * 256 + ((8 * h + r4) ^ (8 * (c4 & 1) + 4 * b16)) * 16 + 8 * (c4 >> 1); asm volatile("" : "+v"(tpw), "+v"(tpr));
    SB;
    ag0 = MFMA(wag0_26, xf26, ag0); PIN1(ag0);
    SB;
    asm volatile("s_waitcnt vmcnt(20)\n\ts_barrier" ::: "memory");
    bf16x4 wag1_0a = TRR((LAS char*)(wb) + 0);
    bf16x4 wag1_0b = TRR((LAS char*)(wb) + 512); bf16x8 wag1_0 = CAT(wag1_0a,wag1_0b);
    DMAI(rsw1, vw, 919296, 122880);
    float K7r = Kr*gq1[3], K7i = Ki*gq1[3];
    PIN2(K7r,K7i);
    float o7r = fmaf(K7r,s7r,-(K7i*s7i)); float o7i = fmaf(-K7r,s7i,-(K7i*s7r));
    PIN2(o7r,o7i);
    SB;
    au0 = MFMA(wau0_26, xf26, au0); PIN1(au0);
    bf16x4 wag1_1a = TRR((LAS char*)(wb) + 2048);
    bf16x4 wag1_1b = TRR((LAS char*)(wb) + 2560); bf16x8 wag1_1 = CAT(wag1_1a,wag1_1b);
    unsigned pr3 = PK(o6r,o7r), pi3 = PK(o6i,o7i);
    PIN2(pr3,pi3);
    *(LAS u32x4*)(LAS char*)(tpw) = u32x4{pr0,pr1,pr2,pr3}; bf16x4 tr0 = TRR(tpr), tr1 = TRR(tpr ^ 64); *(LAS u32x4*)(LAS char*)(tpw) = u32x4{pi0,pi1,pi2,pi3}; bf16x4 ti0 = TRR(tpr), ti1 = TRR(tpr ^ 64);
    SB;
    ag0 = MFMA(wag0_27, xf27, ag0); PIN1(ag0);
    bf16x4 wag1_2a = TRR((LAS char*)(wb) + 4096);
    bf16x4 wag1_2b = TRR((LAS char*)(wb) + 4608); bf16x8 wag1_2 = CAT(wag1_2a,wag1_2b);
    DMAI(rsw1, vw, 1148672, 131072);
    SB;
    au0 = MFMA(wau0_27, xf27, au0); PIN1(au0);
    bf16x4 wag1_3a = TRR((LAS char*)(wb) + 6144);
    bf16x4 wag1_3b = TRR((LAS char*)(wb) + 6656); bf16x8 wag1_3 = CAT(wag1_3a,wag1_3b);
    u32x4 A10_0 = UC(CAT(tr0,tr1)); STX(A10_0, hso + 229376, 0);
    u32x4 A10_1 = UC(CAT(ti0,ti1)); STX(A10_1, hso + 229376, 1);
    SB;
    ag1 = MFMA(wag1_0, xf0, zero16); PIN1(ag1);
    bf16x4 wag1_4a = TRR((LAS char*)(wb) + 8192);
    bf16x4 wag1_4b = TRR((LAS char*)(wb) + 8704); bf16x8 wag1_4 = CAT(wag1_4a,wag1_4b);
    u32x4 rsw2r = rsw2; { unsigned long long b_ = ((unsigned long long)rsw2[1] << 32 | rsw2[0]) + 802816; rsw2r[0] = (unsigned)b_; rsw2r[1] = (unsigned)(b_ >> 32); rsw2r[2] = 802816; } asm volatile("" : "+s"(rsw2r));
    SB;
    ag1 = MFMA(wag1_1, xf1, ag1); PIN1(ag1);
    bf16x4 wag1_5a = TRR((LAS char*)(wb) + 10240);
    bf16x4 wag1_5b = TRR((LAS char*)(wb) + 10752); bf16x8 wag1_5 = CAT(wag1_5a,wag1_5b);
    float t0_0 = ag0[0]*-1.4426950408889634f; float t0_1 = ag0[1]*-1.4426950408889634f; float t0_2 = ag0[2]*-1.4426950408889634f; float t0_3 = ag0[3]*-1.4426950408889634f;
    PIN4(t0_0,t0_1,t0_2,t0_3);
    t0_0 = __builtin_amdgcn_exp2f(t0_0); t0_1 = __builtin_amdgcn_exp2f(t0_1);
    PIN2(t0_0,t0_1);
    SB;
    ag1 = MFMA(wag1_2, xf2, ag1); PIN1(ag1);
    bf16x4 wag1_6a = TRR((LAS char*)(wb) + 12288);
    bf16x4 wag1_6b = TRR((LAS char*)(wb) + 12800); bf16x8 wag1_6 = CAT(wag1_6a,wag1_6b);
    DMAI(rsw1, vw, 1378048, 73728);
    float h0_0 = ag0[0]*au0[0]; float h0_1 = ag0[1]*au0[1];
    PIN2(h0_0,h0_1);
    t0_2 = __builtin_amdgcn_exp2f(t0_2); t0_3 = __builtin_amdgcn_exp2f(t0_3);
    PIN2(t0_2,t0_3);
    SB;
    ag1 = MFMA(wag1_3, xf3, ag1); PIN1(ag1);
    bf16x4 wag1_7a = TRR((LAS char*)(wb) + 14336);
    bf16x4 wag1_7b = TRR((LAS char*)(wb) + 14848); bf16x8 wag1_7 = CAT(wag1_7a,wag1_7b);
    float h0_2 = ag0[2]*au0[2]; float h0_3 = ag0[3]*au0[3];
    PIN2(h0_2,h0_3);
    SB;
    ag1 = MFMA(wag1_4, xf4, ag1); PIN1(ag1);
    SB;
    asm volatile("s_waitcnt vmcnt(8)\n\ts_barrier" ::: "memory");
    bf16x4 wag1_8a = TRR((LAS char*)(wb) + 16384);
    bf16x4 wag1_8b = TRR((LAS char*)(wb) + 16896); bf16x8 wag1_8 = CAT(wag1_8a,wag1_8b);
    DMAI(rsw3, vw, 1792, 81920);
    t0_0 = fmaf(t0_0, cg, cg); t0_1 = fmaf(t0_1, cg, cg); t0_2 = fmaf(t0_2, cg, cg); t0_3 = fmaf(t0_3, cg, cg);
    PIN4(t0_0,t0_1,t0_2,t0_3);
    t0_0 = __builtin_amdgcn_rcpf(t0_0); t0_1 = __builtin_amdgcn_rcpf(t0_1);
    PIN2(t0_0,t0_1);
    SB;
    ag1 = MFMA(wag1_5, xf5, ag1); PIN1(ag1);
    bf16x4 wag1_9a = TRR((LAS char*)(wb) + 18432);
    bf16x4 wag1_9b = TRR((LAS char*)(wb) + 18944); bf16x8 wag1_9 = CAT(wag1_9a,wag1_9b);
    t0_2 = __builtin_amdgcn_rcpf(t0_2); t0_3 = __builtin_amdgcn_rcpf(t0_3);
    PIN2(t0_2,t0_3);
    SB;
    ag1 = MFMA(wag1_6, xf6, ag1); PIN1(ag1);
    bf16x4 wag1_10a = TRR((LAS char*)(wb) + 20480);
    bf16x4 wag1_10b = TRR((LAS char*)(wb) + 20992); bf16x8 wag1_10 = CAT(wag1_10a,wag1_10b);
    DMAI(rsw3, vw, 231168, 139264);
    h0_0 *= t0_0; h0_1 *= t0_1; h0_2 *= t0_2; h0_3 *= t0_3;
    PIN4(h0_0,h0_1,h0_2,h0_3);
    SB;
    ag1 = MFMA(wag1_7, xf7, ag1); PIN1(ag1);
    bf16x4 wag1_11a = TRR((LAS char*)(wb) + 22528);
    bf16x4 wag1_11b = TRR((LAS char*)(wb) + 23040); bf16x8 wag1_11 = CAT(wag1_11a,wag1_11b);
    unsigned k0_0 = PK(h0_0, h0_1), k0_1 = PK(h0_2, h0_3);
    PIN2(k0_0,k0_1);
    float t0_4 = ag0[4]*-1.4426950408889634f; float t0_5 = ag0[5]*-1.4426950408889634f; float t0_6 = ag0[6]*-1.4426950408889634f; float t0_7 = ag0[7]*-1.4426950408889634f;
    PIN4(t0_4,t0_5,t0_6,t0_7);
    SB;
    ag1 = MFMA(wag1_8, xf8, ag1); PIN1(ag1);
    bf16x4 wag1_12a = TRR((LAS char*)(wb) + 24576);
    bf16x4 wag1_12b = TRR((LAS char*)(wb) + 25088); bf16x8 wag1_12 = CAT(wag1_12a,wag1_12b);
    DMAI(rsw3, vw, 460544, 147456);
    t0_4 = __builtin_amdgcn_exp2f(t0_4); t0_5 = __builtin_amdgcn_exp2f(t0_5);
    PIN2(t0_4,t0_5);
    float h0_4 = ag0[4]*au0[4]; float h0_5 = ag0[5]*au0[5];
    PIN2(h0_4,h0_5);
    SB;
    ag1 = MFMA(wag1_9, xf9, ag1); PIN1(ag1);
    bf16x4 wag1_13a = TRR((LAS char*)(wb) + 26624);
    bf16x4 wag1_13b = TRR((LAS char*)(wb) + 27136); bf16x8 wag1_13 = CAT(wag1_13a,wag1_13b);
    t0_6 = __builtin_amdgcn_exp2f(t0_6); t0_7 = __builtin_amdgcn_exp2f(t0_7);
    PIN2(t0_6,t0_7);
    SB;
    ag1 = MFMA(wag1_10, xf10, ag1); PIN1(ag1);
    bf16x4 wag1_14a = TRR((LAS char*)(wb) + 28672);
    bf16x4 wag1_14b = TRR((LAS char*)(wb) + 29184); bf16x8 wag1_14 = CAT(wag1_14a,wag1_14b);
    asm volatile("s_waitcnt vmcnt(4)" ::: "memory");
    STFLAG(fso + 1024);
    DMAI(rsw3, vw, 689920, 90112);
    float h0_6 = ag0[6]*au0[6]; float h0_7 = ag0[7]*au0[7];
    PIN2(h0_6,h0_7);
    t0_4 = fmaf(t0_4, cg, cg); t0_5 = fmaf(t0_5, cg, cg); t0_6 = fmaf(t0_6, cg, cg); t0_7 = fmaf(t0_7, cg, cg);
    PIN4(t0_4,t0_5,t0_6,t0_7);
    SB;
    ag1 = MFMA(wag1_11, xf11, ag1); PIN1(ag1);
    bf16x4 wag1_15a = TRR((LAS char*)(wb) + 30720);
    bf16x4 wag1_15b = TRR((LAS char*)(wb) + 31232); bf16x8 wag1_15 = CAT(wag1_15a,wag1_15b);
    t0_4 = __builtin_amdgcn_rcpf(t0_4); t0_5 = __builtin_amdgcn_rcpf(t0_5);
    PIN2(t0_4,t0_5);
    t0_6 = __builtin_amdgcn_rcpf(t0_6); t0_7 = __builtin_amdgcn_rcpf(t0_7);
    PIN2(t0_6,t0_7);
    SB;
    ag1 = MFMA(wag1_12, xf12, ag1); PIN1(ag1);
    SB;
    asm volatile("s_waitcnt vmcnt(8)\n\ts_barrier" ::: "memory");
    bf16x4 wag1_16a = TRR((LAS char*)(wb) + 32768);
    bf16x4 wag1_16b = TRR((LAS char*)(wb) + 33280); bf16x8 wag1_16 = CAT(wag1_16a,wag1_16b);
    DMAI(rsw3, vw, 919296, 98304);
    h0_4 *= t0_4; h0_5 *= t0_5; h0_6 *= t0_6; h0_7 *= t0_7;
    PIN4(h0_4,h0_5,h0_6,h0_7);
    SB;
    ag1 = MFMA(wag1_13, xf13, ag1); PIN1(ag1);
    bf16x4 wag1_17a = TRR((LAS char*)(wb) + 34816);
    bf16x4 wag1_17b = TRR((LAS char*)(wb) + 35328); bf16x8 wag1_17 = CAT(wag1_17a,wag1_17b);
    unsigned k0_2 = PK(h0_4, h0_5), k0_3 = PK(h0_6, h0_7);
    PIN2(k0_2,k0_3);
    u32x4 A00_0 = {k0_0,k0_1,k0_2,k0_3}; STX(A00_0, hso + 0, 0);
    SB;
    ag1 = MFMA(wag1_14, xf14, ag1); PIN1(ag1);
    bf16x4 wag1_18a = TRR((LAS char*)(wb) + 36864);
    bf16x4 wag1_18b = TRR((LAS char*)(wb) + 37376); bf16x8 wag1_18 = CAT(wag1_18a,wag1_18b);
    float t0_8 = ag0[8]*-1.4426950408889634f; float t0_9 = ag0[9]*-1.4426950408889634f; float t0_10 = ag0[10]*-1.4426950408889634f; float t0_11 = ag0[11]*-1.4426950408889634f;
    PIN4(t0_8,t0_9,t0_10,t0_11);
    t0_8 = __builtin_amdgcn_exp2f(t0_8); t0_9 = __builtin_amdgcn_exp2f(t0_9);
    PIN2(t0_8,t0_9);
    SB;
    ag1 = MFMA(wag1_15, xf15, ag1); PIN1(ag1);
    bf16x4 wag1_19a = TRR((LAS char*)(wb) + 38912);
    bf16x4 wag1_19b = TRR((LAS char*)(wb) + 39424); bf16x8 wag1_19 = CAT(wag1_19a,wag1_19b);
    float h0_8 = ag0[8]*au0[8]; float h0_9 = ag0[9]*au0[9];
    PIN2(h0_8,h0_9);
    t0_10 = __builtin_amdgcn_exp2f(t0_10); t0_11 = __builtin_amdgcn_exp2f(t0_11);
    PIN2(t0_10,t0_11);
    SB;
    ag1 = MFMA(wag1_16, xf16, ag1); PIN1(ag1);
    bf16x4 wag1_20a = TRR((LAS char*)(wb) + 40960);
    bf16x4 wag1_20b = TRR((LAS char*)(wb) + 41472); bf16x8 wag1_20 = CAT(wag1_20a,wag1_20b);
    DMAI(rsw3, vw, 1148672, 106496);
    float h0_10 = ag0[10]*au0[10]; float h0_11 = ag0[11]*au0[11];
    PIN2(h0_10,h0_11);
    SB;
    ag1 = MFMA(wag1_17, xf17, ag1); PIN1(ag1);
    bf16x4 wag1_21a = TRR((LAS char*)(wb) + 43008);
    bf16x4 wag1_21b = TRR((LAS char*)(wb) + 43520); bf16x8 wag1_21 = CAT(wag1_21a,wag1_21b);
    DMAK(rsw2, v2, "s_mul_i32", kq1, 224, 65536);
    t0_8 = fmaf(t0_8, cg, cg); t0_9 = fmaf(t0_9, cg, cg); t0_10 = fmaf(t0_10, cg, cg); t0_11 = fmaf(t0_11, cg, cg);
    PIN4(t0_8,t0_9,t0_10,t0_11);
    t0_8 = __builtin_amdgcn_rcpf(t0_8); t0_9 = __builtin_amdgcn_rcpf(t0_9);
    PIN2(t0_8,t0_9);
    int vf1 = min(l7, 6) * 8; asm volatile("" : "+v"(vf1));
    SB;
    ag1 = MFMA(wag1_18, xf18, ag1); PIN1(ag1);
    bf16x4 wag1_22a = TRR((LAS char*)(wb) + 45056);
    bf16x4 wag1_22b = TRR((LAS char*)(wb) + 45568); bf16x8 wag1_22 = CAT(wag1_22a,wag1_22b);
    t0_10 = __builtin_amdgcn_rcpf(t0_10); t0_11 = __builtin_amdgcn_rcpf(t0_11);
    PIN2(t0_10,t0_11);
    SB;
    ag1 = MFMA(wag1_19, xf19, ag1); PIN1(ag1);
    bf16x4 wag1_23a = TRR((LAS char*)(wb) + 47104);
    bf16x4 wag1_23b = TRR((LAS char*)(wb) + 47616); bf16x8 wag1_23 = CAT(wag1_23a,wag1_23b);
    h0_8 *= t0_8; h0_9 *= t0_9; h0_10 *= t0_10; h0_11 *= t0_11;
    PIN4(h0_8,h0_9,h0_10,h0_11);
    SB;
    ag1 = MFMA(wag1_20, xf20, ag1); PIN1(ag1);
    SB;
    asm volatile("s_waitcnt vmcnt(8)\n\ts_barrier" ::: "memory");
    wb += -16384; PIN1(wb);
    bf16x4 wag1_24a = TRR((LAS char*)(wb) + 0);
    bf16x4 wag1_24b = TRR((LAS char*)(wb) + 512); bf16x8 wag1_24 = CAT(wag1_24a,wag1_24b);
    DMAI(rsw3, vw, 1378048, 114688);
    unsigned k0_4 = PK(h0_8, h0_9), k0_5 = PK(h0_10, h0_11);
    PIN2(k0_4,k0_5);
    float t0_12 = ag0[12]*-1.4426950408889634f; float t0_13 = ag0[13]*-1.4426950408889634f; float t0_14 = ag0[14]*-1.4426950408889634f; float t0_15 = ag0[15]*-1.4426950408889634f;
    PIN4(t0_12,t0_13,t0_14,t0_15);
    SB;
    ag1 = MFMA(wag1_21, xf21, ag1); PIN1(ag1);
    bf16x4 wag1_25a = TRR((LAS char*)(wb) + 2048);
    bf16x4 wag1_25b = TRR((LAS char*)(wb) + 2560); bf16x8 wag1_25 = CAT(wag1_25a,wag1_25b);
    DMAK(rsw2r, v2, "s_mul_i32", kq5, 224, 122880);
    t0_12 = __builtin_amdgcn_exp2f(t0_12); t0_13 = __builtin_amdgcn_exp2f(t0_13);
    PIN2(t0_12,t0_13);
    float h0_12 = ag0[12]*au0[12]; float h0_13 = ag0[13]*au0[13];
    PIN2(h0_12,h0_13);
    SB;
    ag1 = MFMA(wag1_22, xf22, ag1); PIN1(ag1);
    bf16x4 wag1_26a = TRR((LAS char*)(wb) + 4096);
    bf16x4 wag1_26b = TRR((LAS char*)(wb) + 4608); bf16x8 wag1_26 = CAT(wag1_26a,wag1_26b);
    u32x2 fv1; LDFLAG(fv1, vf1, fpl);
    t0_14 = __builtin_amdgcn_exp2f(t0_14); t0_15 = __builtin_amdgcn_exp2f(t0_15);
    PIN2(t0_14,t0_15);
    SB;
    ag1 = MFMA(wag1_23, xf23, ag1); PIN1(ag1);
    bf16x4 wag1_27a = TRR((LAS char*)(wb) + 6144);
    bf16x4 wag1_27b = TRR((LAS char*)(wb) + 6656); bf16x8 wag1_27 = CAT(wag1_27a,wag1_27b);
    float h0_14 = ag0[14]*au0[14]; float h0_15 = ag0[15]*au0[15];
    PIN2(h0_14,h0_15);
    t0_12 = fmaf(t0_12, cg, cg); t0_13 = fmaf(t0_13, cg, cg); t0_14 = fmaf(t0_14, cg, cg); t0_15 = fmaf(t0_15, cg, cg);
    PIN4(t0_12,t0_13,t0_14,t0_15);
    SB;
    ag1 = MFMA(wag1_24, xf24, ag1); PIN1(ag1);
    bf16x4 wau1_0a = TRR((LAS char*)(wb) + 8192);
    bf16x4 wau1_0b = TRR((LAS char*)(wb) + 8704); bf16x8 wau1_0 = CAT(wau1_0a,wau1_0b);
    t0_12 = __builtin_amdgcn_rcpf(t0_12); t0_13 = __builtin_amdgcn_rcpf(t0_13);
    PIN2(t0_12,t0_13);
    t0_14 = __builtin_amdgcn_rcpf(t0_14); t0_15 = __builtin_amdgcn_rcpf(t0_15);
    PIN2(t0_14,t0_15);
    int hb0 = hl1 - 229376, hbx = hb0 + (mu ? -114688 : 114688), hbn = keep ? hbx : hb0; int hn1 = hbn + (keep ? kq4 : kq1) * 16; int hn2 = hbn + (keep ? kq5 : kq2) * 16; int hn3 = hbn + (keep ? kq6 : kq3) * 16; asm volatile("" : "+s"(hn1), "+s"(hn2), "+s"(hn3));
    SB;
    ag1 = MFMA(wag1_25, xf25, ag1); PIN1(ag1);
    bf16x4 wau1_1a = TRR((LAS char*)(wb) + 10240);
    bf16x4 wau1_1b = TRR((LAS char*)(wb) + 10752); bf16x8 wau1_1 = CAT(wau1_1a,wau1_1b);
    h0_12 *= t0_12; h0_13 *= t0_13; h0_14 *= t0_14; h0_15 *= t0_15;
    PIN4(h0_12,h0_13,h0_14,h0_15);
    SB;
    ag1 = MFMA(wag1_26, xf26, ag1); PIN1(ag1);
    bf16x4 wau1_2a = TRR((LAS char*)(wb) + 12288);
    bf16x4 wau1_2b = TRR((LAS char*)(wb) + 12800); bf16x8 wau1_2 = CAT(wau1_2a,wau1_2b);
    unsigned k0_6 = PK(h0_12, h0_13), k0_7 = PK(h0_14, h0_15);
    PIN2(k0_6,k0_7);
    u32x4 A00_1 = {k0_4,k0_5,k0_6,k0_7}; STX(A00_1, hso + 0, 1);
    int hbk = keep ? hb0 : hbx; int hk1 = hbk + (keep ? kq1 : kq4) * 16; int hk2 = hbk + (keep ? kq2 : kq5) * 16; int hk3 = hbk + (keep ? kq3 : kq6) * 16; asm volatile("" : "+s"(hk1), "+s"(hk2), "+s"(hk3));
    SB;
    ag1 = MFMA(wag1_27, xf27, ag1); PIN1(ag1);
    bf16x4 wau1_3a = TRR((LAS char*)(wb) + 14336);
    bf16x4 wau1_3b = TRR((LAS char*)(wb) + 14848); bf16x8 wau1_3 = CAT(wau1_3a,wau1_3b);
    SB;
    au1 = MFMA(wau1_0, xf0, zero16); PIN1(au1);
    SB;
    asm volatile("s_waitcnt vmcnt(10)\n\ts_barrier" ::: "memory");
    wb += 16384; PIN1(wb);
    bf16x4 wau1_4a = TRR((LAS char*)(wb) + 49152);
    bf16x4 wau1_4b = TRR((LAS char*)(wb) + 49664); bf16x8 wau1_4 = CAT(wau1_4a,wau1_4b);
    DMAK(rsw2, v2, "s_mul_i32", kq4, 224, 73728);
    SB;
    au1 = MFMA(wau1_1, xf1, au1); PIN1(au1);
    bf16x4 wau1_5a = TRR((LAS char*)(wb) + 51200);
    bf16x4 wau1_5b = TRR((LAS char*)(wb) + 51712); bf16x8 wau1_5 = CAT(wau1_5a,wau1_5b);
    DMAK(rsw2, v2, "s_mul_i32", kq2, 224, 131072);
    float t1_0 = ag1[0]*-1.4426950408889634f; float t1_1 = ag1[1]*-1.4426950408889634f;
    PIN2(t1_0,t1_1);
    float t1_2 = ag1[2]*-1.4426950408889634f; float t1_3 = ag1[3]*-1.4426950408889634f;
    PIN2(t1_2,t1_3);
    t1_0 = __builtin_amdgcn_exp2f(t1_0); t1_1 = __builtin_amdgcn_exp2f(t1_1);
    PIN2(t1_0,t1_1);
    SB;
    au1 = MFMA(wau1_2, xf2, au1); PIN1(au1);
    bf16x4 wau1_6a = TRR((LAS char*)(wb) + 53248);
    bf16x4 wau1_6b = TRR((LAS char*)(wb) + 53760); bf16x8 wau1_6 = CAT(wau1_6a,wau1_6b);
    t1_2 = __builtin_amdgcn_exp2f(t1_2); t1_3 = __builtin_amdgcn_exp2f(t1_3);
    PIN2(t1_2,t1_3);
    SB;
    au1 = MFMA(wau1_3, xf3, au1); PIN1(au1);
    bf16x4 wau1_7a = TRR((LAS char*)(wb) + 55296);
    bf16x4 wau1_7b = TRR((LAS char*)(wb) + 55808); bf16x8 wau1_7 = CAT(wau1_7a,wau1_7b);
    asm volatile("s_waitcnt vmcnt(3)" : "+v"(fv1));
    if (__builtin_expect(ANY(U64(fv1) != tag64), 0)) { for (int sp = 0; sp < SPINMAX; ++sp) { __builtin_amdgcn_s_sleep(1); LDFLAG(fv1, vf1, fpl); asm volatile("s_waitcnt vmcnt(0)" : "+v"(fv1)); if (!(ANY(U64(fv1) != tag64))) break; } }
    u32x4 A11_0; LDV(A11_0, l16, hl1 + kq1*16, 0);
    t1_0 = fmaf(t1_0, cg, cg); t1_1 = fmaf(t1_1, cg, cg);
    PIN2(t1_0,t1_1);
    SB;
    au1 = MFMA(wau1_4, xf4, au1); PIN1(au1);
    bf16x4 wau1_8a = TRR((LAS char*)(wb) + 57344);
    bf16x4 wau1_8b = TRR((LAS char*)(wb) + 57856); bf16x8 wau1_8 = CAT(wau1_8a,wau1_8b);
    t1_2 = fmaf(t1_2, cg, cg); t1_3 = fmaf(t1_3, cg, cg);
    PIN2(t1_2,t1_3);
    t1_0 = __builtin_amdgcn_rcpf(t1_0); t1_1 = __builtin_amdgcn_rcpf(t1_1);
    PIN2(t1_0,t1_1);
    SB;
    au1 = MFMA(wau1_5, xf5, au1); PIN1(au1);
    bf16x4 wau1_9a = TRR((LAS char*)(wb) + 59392);
    bf16x4 wau1_9b = TRR((LAS char*)(wb) + 59904); bf16x8 wau1_9 = CAT(wau1_9a,wau1_9b);
    u32x4 A11_1; LDV(A11_1, l16, hl1 + kq1*16, 1);
    t1_2 = __builtin_amdgcn_rcpf(t1_2); t1_3 = __builtin_amdgcn_rcpf(t1_3);
    PIN2(t1_2,t1_3);
    SB;
    au1 = MFMA(wau1_6, xf6, au1); PIN1(au1);
    bf16x4 wau1_10a = TRR((LAS char*)(wb) + 61440);
    bf16x4 wau1_10b = TRR((LAS char*)(wb) + 61952); bf16x8 wau1_10 = CAT(wau1_10a,wau1_10b);
    t1_0 = fmul1(t1_0, ag1[0]); t1_1 = fmul1(t1_1, ag1[1]);
    PIN2(t1_0,t1_1);
    SB;
    au1 = MFMA(wau1_7, xf7, au1); PIN1(au1);
    bf16x4 wau1_11a = TRR((LAS char*)(wb) + 63488);
    bf16x4 wau1_11b = TRR((LAS char*)(wb) + 64000); bf16x8 wau1_11 = CAT(wau1_11a,wau1_11b);
    u32x4 A12_0; LDV(A12_0, l16, hl1 + kq2*16, 0);
    t1_2 = fmul1(t1_2, ag1[2]); t1_3 = fmul1(t1_3, ag1[3]);
    PIN2(t1_2,t1_3);
    float t1_4 = ag1[4]*-1.4426950408889634f; float t1_5 = ag1[5]*-1.4426950408889634f;
    PIN2(t1_4,t1_5);
    float t1_6 = ag1[6]*-1.4426950408889634f; float t1_7 = ag1[7]*-1.4426950408889634f;
    PIN2(t1_6,t1_7);
    SB;
    au1 = MFMA(wau1_8, xf8, au1); PIN1(au1);
    SB;
    asm volatile("s_waitcnt vmcnt(12)\n\ts_barrier" ::: "memory");
    bf16x4 wau1_12a = TRR((LAS char*)(wb) + 0);
    bf16x4 wau1_12b = TRR((LAS char*)(wb) + 512); bf16x8 wau1_12 = CAT(wau1_12a,wau1_12b);
    asm volatile("s_waitcnt vmcnt(5)" ::: "memory");
    STFLAG(fso + 0);
    t1_4 = __builtin_amdgcn_exp2f(t1_4); t1_5 = __builtin_amdgcn_exp2f(t1_5);
    PIN2(t1_4,t1_5);
    SB;
    au1 = MFMA(wau1_9, xf9, au1); PIN1(au1);
    bf16x4 wau1_13a = TRR((LAS char*)(wb) + 2048);
    bf16x4 wau1_13b = TRR((LAS char*)(wb) + 2560); bf16x8 wau1_13 = CAT(wau1_13a,wau1_13b);
    u32x4 A12_1; LDV(A12_1, l16, hl1 + kq2*16, 1);
    t1_6 = __builtin_amdgcn_exp2f(t1_6); t1_7 = __builtin_amdgcn_exp2f(t1_7);
    PIN2(t1_6,t1_7);
    SB;
    au1 = MFMA(wau1_10, xf10, au1); PIN1(au1);
    bf16x4 wau1_14a = TRR((LAS char*)(wb) + 4096);
    bf16x4 wau1_14b = TRR((LAS char*)(wb) + 4608); bf16x8 wau1_14 = CAT(wau1_14a,wau1_14b);
    u32x4 A13_0; LDV(A13_0, l16, hl1 + kq3*16, 0);
    t1_4 = fmaf(t1_4, cg, cg); t1_5 = fmaf(t1_5, cg, cg);
    PIN2(t1_4,t1_5);
    t1_6 = fmaf(t1_6, cg, cg); t1_7 = fmaf(t1_7, cg, cg);
    PIN2(t1_6,t1_7);
    SB;
    au1 = MFMA(wau1_11, xf11, au1); PIN1(au1);
    bf16x4 wau1_15a = TRR((LAS char*)(wb) + 6144);
    bf16x4 wau1_15b = TRR((LAS char*)(wb) + 6656); bf16x8 wau1_15 = CAT(wau1_15a,wau1_15b);
    t1_4 = __builtin_amdgcn_rcpf(t1_4); t1_5 = __builtin_amdgcn_rcpf(t1_5);
    PIN2(t1_4,t1_5);
    SB;
    au1 = MFMA(wau1_12, xf12, au1); PIN1(au1);
    bf16x4 wau1_16a = TRR((LAS char*)(wb) + 8192);
    bf16x4 wau1_16b = TRR((LAS char*)(wb) + 8704); bf16x8 wau1_16 = CAT(wau1_16a,wau1_16b);
    u32x4 A13_1; LDV(A13_1, l16, hl1 + kq3*16, 1);
    t1_6 = __builtin_amdgcn_rcpf(t1_6); t1_7 = __builtin_amdgcn_rcpf(t1_7);
    PIN2(t1_6,t1_7);
    SB;
    au1 = MFMA(wau1_13, xf13, au1); PIN1(au1);
    bf16x4 wau1_17a = TRR((LAS char*)(wb) + 10240);
    bf16x4 wau1_17b = TRR((LAS char*)(wb) + 10752); bf16x8 wau1_17 = CAT(wau1_17a,wau1_17b);
    t1_4 = fmul1(t1_4, ag1[4]); t1_5 = fmul1(t1_5, ag1[5]);
    PIN2(t1_4,t1_5);
    t1_6 = fmul1(t1_6, ag1[6]); t1_7 = fmul1(t1_7, ag1[7]);
    PIN2(t1_6,t1_7);
    SB;
    au1 = MFMA(wau1_14, xf14, au1); PIN1(au1);
    bf16x4 wau1_18a = TRR((LAS char*)(wb) + 12288);
    bf16x4 wau1_18b = TRR((LAS char*)(wb) + 12800); bf16x8 wau1_18 = CAT(wau1_18a,wau1_18b);
    u32x4 A14_0; LDV(A14_0, l16, hl1 + kq4*16, 0);
    float t1_8 = ag1[8]*-1.4426950408889634f; float t1_9 = ag1[9]*-1.4426950408889634f;
    PIN2(t1_8,t1_9);
    float t1_10 = ag1[10]*-1.4426950408889634f; float t1_11 = ag1[11]*-1.4426950408889634f;
    PIN2(t1_10,t1_11);
    t1_8 = __builtin_amdgcn_exp2f(t1_8); t1_9 = __builtin_amdgcn_exp2f(t1_9);
    PIN2(t1_8,t1_9);
    SB;
    au1 = MFMA(wau1_15, xf15, au1); PIN1(au1);
    bf16x4 wau1_19a = TRR((LAS char*)(wb) + 14336);
    bf16x4 wau1_19b = TRR((LAS char*)(wb) + 14848); bf16x8 wau1_19 = CAT(wau1_19a,wau1_19b);
    t1_10 = __builtin_amdgcn_exp2f(t1_10); t1_11 = __builtin_amdgcn_exp2f(t1_11);
    PIN2(t1_10,t1_11);
    int vf0 = (l7 == 7 ? 135 - 14 * rh + ct : l7) * 8 + (lane & 8 ? 1024 - 1024 * mu : 512); asm volatile("" : "+v"(vf0));
    SB;
    au1 = MFMA(wau1_16, xf16, au1); PIN1(au1);
    SB;
    asm volatile("s_waitcnt vmcnt(13)\n\ts_barrier" ::: "memory");
    bf16x4 wau1_20a = TRR((LAS char*)(wb) + 16384);
    bf16x4 wau1_20b = TRR((LAS char*)(wb) + 16896); bf16x8 wau1_20 = CAT(wau1_20a,wau1_20b);
    u32x4 A14_1; LDV(A14_1, l16, hl1 + kq4*16, 1);
    t1_8 = fmaf(t1_8, cg, cg); t1_9 = fmaf(t1_9, cg, cg);
    PIN2(t1_8,t1_9);
    SB;
    au1 = MFMA(wau1_17, xf17, au1); PIN1(au1);
    bf16x4 wau1_21a = TRR((LAS char*)(wb) + 18432);
    bf16x4 wau1_21b = TRR((LAS char*)(wb) + 18944); bf16x8 wau1_21 = CAT(wau1_21a,wau1_21b);
    t1_10 = fmaf(t1_10, cg, cg); t1_11 = fmaf(t1_11, cg, cg);
    PIN2(t1_10,t1_11);
    t1_8 = __builtin_amdgcn_rcpf(t1_8); t1_9 = __builtin_amdgcn_rcpf(t1_9);
    PIN2(t1_8,t1_9);
    SB;
    au1 = MFMA(wau1_18, xf18, au1); PIN1(au1);
    bf16x4 wau1_22a = TRR((LAS char*)(wb) + 20480);
    bf16x4 wau1_22b = TRR((LAS char*)(wb) + 20992); bf16x8 wau1_22 = CAT(wau1_22a,wau1_22b);
    u32x4 A15_0; LDV(A15_0, l16, hl1 + kq5*16, 0);
    t1_10 = __builtin_amdgcn_rcpf(t1_10); t1_11 = __builtin_amdgcn_rcpf(t1_11);
    PIN2(t1_10,t1_11);
    SB;
    au1 = MFMA(wau1_19, xf19, au1); PIN1(au1);
    bf16x4 wau1_23a = TRR((LAS char*)(wb) + 22528);
    bf16x4 wau1_23b = TRR((LAS char*)(wb) + 23040); bf16x8 wau1_23 = CAT(wau1_23a,wau1_23b);
    DMAK(rsw2, v2, "s_mul_i32", kq5, 224, 139264);
    t1_8 = fmul1(t1_8, ag1[8]); t1_9 = fmul1(t1_9, ag1[9]);
    PIN2(t1_8,t1_9);
    SB;
    au1 = MFMA(wau1_20, xf20, au1); PIN1(au1);
    bf16x4 wau1_24a = TRR((LAS char*)(wb) + 24576);
    bf16x4 wau1_24b = TRR((LAS char*)(wb) + 25088); bf16x8 wau1_24 = CAT(wau1_24a,wau1_24b);
    t1_10 = fmul1(t1_10, ag1[10]); t1_11 = fmul1(t1_11, ag1[11]);
    PIN2(t1_10,t1_11);
    float t1_12 = ag1[12]*-1.4426950408889634f; float t1_13 = ag1[13]*-1.4426950408889634f;
    PIN2(t1_12,t1_13);
    float t1_14 = ag1[14]*-1.4426950408889634f; float t1_15 = ag1[15]*-1.4426950408889634f;
    PIN2(t1_14,t1_15);
    SB;
    au1 = MFMA(wau1_21, xf21, au1); PIN1(au1);
    bf16x4 wau1_25a = TRR((LAS char*)(wb) + 26624);
    bf16x4 wau1_25b = TRR((LAS char*)(wb) + 27136); bf16x8 wau1_25 = CAT(wau1_25a,wau1_25b);
    u32x4 A15_1; LDV(A15_1, l16, hl1 + kq5*16, 1);
    t1_12 = __builtin_amdgcn_exp2f(t1_12); t1_13 = __builtin_amdgcn_exp2f(t1_13);
    PIN2(t1_12,t1_13);
    unsigned cbz = sb + i32 * 64 + 16 * ((2 * ni + h) ^ ((i32 >> 2) & 3)); asm volatile("" : "+v"(cbz));
    SB;
    au1 = MFMA(wau1_22, xf22, au1); PIN1(au1);
    bf16x4 wau1_26a = TRR((LAS char*)(wb) + 28672);
    bf16x4 wau1_26b = TRR((LAS char*)(wb) + 29184); bf16x8 wau1_26 = CAT(wau1_26a,wau1_26b);
    t1_14 = __builtin_amdgcn_exp2f(t1_14); t1_15 = __builtin_amdgcn_exp2f(t1_15);
    PIN2(t1_14,t1_15);
    SB;
    au1 = MFMA(wau1_23, xf23, au1); PIN1(au1);
    bf16x4 wau1_27a = TRR((LAS char*)(wb) + 30720);
    bf16x4 wau1_27b = TRR((LAS char*)(wb) + 31232); bf16x8 wau1_27 = CAT(wau1_27a,wau1_27b);
    u32x4 A16_0; LDV(A16_0, l16, hl1 + kq6*16, 0);
    t1_12 = fmaf(t1_12, cg, cg); t1_13 = fmaf(t1_13, cg, cg);
    PIN2(t1_12,t1_13);
    t1_14 = fmaf(t1_14, cg, cg); t1_15 = fmaf(t1_15, cg, cg);
    PIN2(t1_14,t1_15);
    SB;
    au1 = MFMA(wau1_24, xf24, au1); PIN1(au1);
    SB;
    asm volatile("s_waitcnt vmcnt(47)\n\ts_barrier" ::: "memory");
    unsigned cba = cbz + 0; PIN1(cba);
    bf16x8 z1o0_00 = RD128((LAS char*)(cba) + 0);
    u32x2 fv0; LDFLAG(fv0, vf0, fpl - 1536);
    t1_12 = __builtin_amdgcn_rcpf(t1_12); t1_13 = __builtin_amdgcn_rcpf(t1_13);
    PIN2(t1_12,t1_13);
    SB;
    au1 = MFMA(wau1_25, xf25, au1); PIN1(au1);
    bf16x8 z1o0_01 = RD128((LAS char*)(cba) + 2048);
    u32x4 A16_1; LDV(A16_1, l16, hl1 + kq6*16, 1);
    t1_14 = __builtin_amdgcn_rcpf(t1_14); t1_15 = __builtin_amdgcn_rcpf(t1_15);
    PIN2(t1_14,t1_15);
    SB;
    au1 = MFMA(wau1_26, xf26, au1); PIN1(au1);
    bf16x8 z1o0_10 = RD128((LAS char*)(cba) + 4096);
    t1_12 = fmul1(t1_12, ag1[12]); t1_13 = fmul1(t1_13, ag1[13]);
    PIN2(t1_12,t1_13);
    t1_14 = fmul1(t1_14, ag1[14]); t1_15 = fmul1(t1_15, ag1[15]);
    PIN2(t1_14,t1_15);
    SB;
    au1 = MFMA(wau1_27, xf27, au1); PIN1(au1);
    bf16x8 z1o0_11 = RD128((LAS char*)(cba) + 6144);
    DMAK(rsw2, v2, "s_mul_i32", kq3, 224, 81920);
    SB;
    ap0 = MFMA(BC(A10_0), z1o0_00, zero16); PIN1(ap0);
    bf16x8 z1o1_00 = RD128((LAS char*)(cba) + 8192);
    DMAK(rsw2, v2, "s_mul_i32", kq6, 224, 90112);
    SB;
    ap1 = MFMA(BC(A10_0), z1o0_01, zero16); PIN1(ap1);
    SB;
    bf16x8 z1o1_01 = RD128((LAS char*)(cba) + 10240);
    float h1_0 = t1_0*au1[0]; float h1_1 = t1_1*au1[1]; float h1_2 = t1_2*au1[2]; float h1_3 = t1_3*au1[3];
    PIN4(h1_0,h1_1,h1_2,h1_3);
    unsigned k1_0 = PK(h1_0, h1_1), k1_1 = PK(h1_2, h1_3);
    PIN2(k1_0,k1_1);
    float h1_4 = t1_4*au1[4]; float h1_5 = t1_5*au1[5]; float h1_6 = t1_6*au1[6]; float h1_7 = t1_7*au1[7];
    PIN4(h1_4,h1_5,h1_6,h1_7);
    SB;
    ap0 = MFMA(BC(A10_1), z1o0_10, ap0); PIN1(ap0);
    bf16x8 z1o1_10 = RD128((LAS char*)(cba) + 12288);
    unsigned k1_2 = PK(h1_4, h1_5), k1_3 = PK(h1_6, h1_7);
    PIN2(k1_2,k1_3);
    u32x4 A20_0 = {k1_0,k1_1,k1_2,k1_3}; STX(A20_0, hso + 458752, 0);
    float h1_8 = t1_8*au1[8]; float h1_9 = t1_9*au1[9]; float h1_10 = t1_10*au1[10]; float h1_11 = t1_11*au1[11];
    PIN4(h1_8,h1_9,h1_10,h1_11);
    unsigned k1_4 = PK(h1_8, h1_9), k1_5 = PK(h1_10, h1_11);
    PIN2(k1_4,k1_5);
    SB;
    ap1 = MFMA(BC(A10_1), z1o0_11, ap1); PIN1(ap1);
    bf16x8 z1o1_11 = RD128((LAS char*)(cba) + 14336);
    float h1_12 = t1_12*au1[12]; float h1_13 = t1_13*au1[13]; float h1_14 = t1_14*au1[14]; float h1_15 = t1_15*au1[15];
    PIN4(h1_12,h1_13,h1_14,h1_15);
    unsigned k1_6 = PK(h1_12, h1_13), k1_7 = PK(h1_14, h1_15);
    PIN2(k1_6,k1_7);
    u32x4 A20_1 = {k1_4,k1_5,k1_6,k1_7}; STX(A20_1, hso + 458752, 1);
    SB;
    asm volatile("s_waitcnt vmcnt(18)" : "+v"(A11_0));
    ap0 = MFMA(BC(A11_0), z1o1_00, ap0); PIN1(ap0);
    bf16x8 z1o2_00 = RD128((LAS char*)(cba) + 16384);
    SB;
    ap1 = MFMA(BC(A11_0), z1o1_01, ap1); PIN1(ap1);
    bf16x8 z1o2_01 = RD128((LAS char*)(cba) + 18432);
    SB;
    asm volatile("s_waitcnt vmcnt(17)" : "+v"(A11_1));
    ap0 = MFMA(BC(A11_1), z1o1_10, ap0); PIN1(ap0);
    bf16x8 z1o2_10 = RD128((LAS char*)(cba) + 20480);
    asm volatile("s_waitcnt vmcnt(5)" : "+v"(fv0));
    if (__builtin_expect(ANY(U64(fv0) != tag64), 0)) { for (int sp = 0; sp < SPINMAX; ++sp) { __builtin_amdgcn_s_sleep(1); LDFLAG(fv0, vf0, fpl - 1536); asm volatile("s_waitcnt vmcnt(0)" : "+v"(fv0)); if (!(ANY(U64(fv0) != tag64))) break; } }
    u32x4 A0n1_0; LDV(A0n1_0, l16, hn1, 0);
    SB;
    ap1 = MFMA(BC(A11_1), z1o1_11, ap1); PIN1(ap1);
    bf16x8 z1o2_11 = RD128((LAS char*)(cba) + 22528);
    SB;
    asm volatile("s_waitcnt vmcnt(17)" : "+v"(A12_0));
    ap0 = MFMA(BC(A12_0), z1o2_00, ap0); PIN1(ap0);
    bf16x8 z1o3_00 = RD128((LAS char*)(cba) + 24576);
    float gt0 = *(LAS float*)((LAS char*)smem + LM + 2048 + tk0*4), gt1 = *(LAS float*)((LAS char*)smem + LM + 2048 + (tk0 ^ 16)*4);
    u32x4 A0n1_1; LDV(A0n1_1, l16, hn1, 1);
    SB;
    ap1 = MFMA(BC(A12_0), z1o2_01, ap1); PIN1(ap1);
    bf16x8 z1o3_01 = RD128((LAS char*)(cba) + 26624);
    SB;
    asm volatile("s_waitcnt vmcnt(16)" : "+v"(A12_1));
    ap0 = MFMA(BC(A12_1), z1o2_10, ap0); PIN1(ap0);
    bf16x8 z1o3_10 = RD128((LAS char*)(cba) + 28672);
    u32x4 A0n2_0; LDV(A0n2_0, l16, hn2, 0);
    asm volatile("s_waitcnt vmcnt(42)" : "+v"(xe0), "+v"(xe1), "+v"(de));
    float d0 = blo(de[0]), d1 = bhi(de[0]), d2 = blo(de[1]), d3 = bhi(de[1]);
    PIN4(d0,d1,d2,d3);
    f32x4 sk0 = {fmul1(gt0,fmul1(blo(xe0[0]),d0)), fmul1(gt0,fmul1(bhi(xe0[0]),d1)), fmul1(gt0,fmul1(blo(xe0[1]),d2)), fmul1(gt0,fmul1(bhi(xe0[1]),d3))};
    PIN1(sk0);
    unsigned w2z = sb + ni * 4096 + (8 * h + r4) * 128 + 32 * b16 + 8 * c4 + 64 * (r4 >> 1); int kpo = keep ? 8192 : 0; asm volatile("" : "+s"(kpo), "+v"(w2z));
    SB;
    ap1 = MFMA(BC(A12_1), z1o2_11, ap1); PIN1(ap1);
    bf16x8 z1o3_11 = RD128((LAS char*)(cba) + 30720);
    SB;
    asm volatile("s_waitcnt vmcnt(16)" : "+v"(A13_0));
    ap0 = MFMA(BC(A13_0), z1o3_00, ap0); PIN1(ap0);
    SB;
    asm volatile("s_waitcnt vmcnt(52)\n\ts_barrier" ::: "memory");
    bf16x8 z1o4_00 = RD128((LAS char*)(cba) + 32768);
    DMAK(rsw2r, v2, "s_mul_i32", kq0, 224, 0);
    f32x4 sk1 = {fmul1(gt1,fmul1(blo(xe1[0]),d0)), fmul1(gt1,fmul1(bhi(xe1[0]),d1)), fmul1(gt1,fmul1(blo(xe1[1]),d2)), fmul1(gt1,fmul1(bhi(xe1[1]),d3))};
    PIN1(sk1);
    SB;
    ap1 = MFMA(BC(A13_0), z1o3_01, ap1); PIN1(ap1);
    bf16x8 z1o4_01 = RD128((LAS char*)(cba) + 34816);
    SB;
    asm volatile("s_waitcnt vmcnt(16)" : "+v"(A13_1));
    ap0 = MFMA(BC(A13_1), z1o3_10, ap0); PIN1(ap0);
    bf16x8 z1o4_10 = RD128((LAS char*)(cba) + 36864);
    u32x4 A0n2_1; LDV(A0n2_1, l16, hn2, 1);
    int l16r = l16 + 458752, vo2 = 2 * vxe, vo3 = vo2 + (st & 8 ? -28672 : 28672), pso = pli ^ 16384; asm volatile("" : "+s"(pso), "+v"(l16r), "+v"(vo2), "+v"(vo3));
    SB;
    ap1 = MFMA(BC(A13_1), z1o3_11, ap1); PIN1(ap1);
    bf16x8 z1o4_11 = RD128((LAS char*)(cba) + 38912);
    SB;
    asm volatile("s_waitcnt vmcnt(16)" : "+v"(A14_0));
    ap0 = MFMA(BC(A14_0), z1o4_00, ap0); PIN1(ap0);
    bf16x8 z1o5_00 = RD128((LAS char*)(cba) + 40960);
    u32x4 A0n3_0; LDV(A0n3_0, l16, hn3, 0);
    SB;
    ap1 = MFMA(BC(A14_0), z1o4_01, ap1); PIN1(ap1);
    bf16x8 z1o5_01 = RD128((LAS char*)(cba) + 43008);
    SB;
    asm volatile("s_waitcnt vmcnt(16)" : "+v"(A14_1));
    ap0 = MFMA(BC(A14_1), z1o4_10, ap0); PIN1(ap0);
    bf16x8 z1o5_10 = RD128((LAS char*)(cba) + 45056);
    DMAK(rsw2r, v2, "s_mul_i32", kq1, 224, 98304);
    SB;
    ap1 = MFMA(BC(A14_1), z1o4_11, ap1); PIN1(ap1);
    bf16x8 z1o5_11 = RD128((LAS char*)(cba) + 47104);
    SB;
    asm volatile("s_waitcnt vmcnt(16)" : "+v"(A15_0));
    ap0 = MFMA(BC(A15_0), z1o5_00, ap0); PIN1(ap0);
    bf16x8 z1o6_00 = RD128((LAS char*)(cba) + 49152);
    DMAK(rsw2r, v2, "s_mul_i32", kq4, 224, 106496);
    SB;
    ap1 = MFMA(BC(A15_0), z1o5_01, ap1); PIN1(ap1);
    bf16x8 z1o6_01 = RD128((LAS char*)(cba) + 51200);
    SB;
    asm volatile("s_waitcnt vmcnt(15)" : "+v"(A15_1));
    ap0 = MFMA(BC(A15_1), z1o5_10, ap0); PIN1(ap0);
    bf16x8 z1o6_10 = RD128((LAS char*)(cba) + 53248);
    DMAK(rsw2r, v2, "s_mul_i32", kq2, 224, 114688);
    SB;
    ap1 = MFMA(BC(A15_1), z1o5_11, ap1); PIN1(ap1);
    bf16x8 z1o6_11 = RD128((LAS char*)(cba) + 55296);
    SB;
    asm volatile("s_waitcnt vmcnt(15)" : "+v"(A16_0));
    ap0 = MFMA(BC(A16_0), z1o6_00, ap0); PIN1(ap0);
    SB;
    asm volatile("s_waitcnt vmcnt(5)\n\ts_barrier" ::: "memory");
    unsigned w2a = w2z + 0; PIN1(w2a);
    bf16x4 z0o0_00a = TRR((LAS char*)(w2a) + 57344);
    bf16x4 z0o0_00b = TRR((LAS char*)(w2a) + 57856); bf16x8 z0o0_00 = CAT(z0o0_00a,z0o0_00b);
    DMAK(rsw2r, v2, "s_mul_i32", kq3, 224, 147456);
    SB;
    ap1 = MFMA(BC(A16_0), z1o6_01, ap1); PIN1(ap1);
    unsigned w2b = (w2z ^ 64) + 0; PIN1(w2b);
    bf16x4 z0o0_01a = TRR((LAS char*)(w2b) + 57344);
    bf16x4 z0o0_01b = TRR((LAS char*)(w2b) + 57856); bf16x8 z0o0_01 = CAT(z0o0_01a,z0o0_01b);
    SB;
    asm volatile("s_waitcnt vmcnt(14)" : "+v"(A16_1));
    ap0 = MFMA(BC(A16_1), z1o6_10, ap0); PIN1(ap0);
    bf16x4 z0o0_10a = TRR((LAS char*)(w2a) + 59392);
    bf16x4 z0o0_10b = TRR((LAS char*)(w2a) + 59904); bf16x8 z0o0_10 = CAT(z0o0_10a,z0o0_10b);
    DMAK(rsw2r, v2, "s_mul_i32", kq6, 224, 155648);
    SB;
    ap1 = MFMA(BC(A16_1), z1o6_11, ap1); PIN1(ap1);
    bf16x4 z0o0_11a = TRR((LAS char*)(w2b) + 59392);
    bf16x4 z0o0_11b = TRR((LAS char*)(w2b) + 59904); bf16x8 z0o0_11 = CAT(z0o0_11a,z0o0_11b);
    SB;
    ap0 = MFMA(BC(A00_0), z0o0_00, ap0); PIN1(ap0);
    bf16x4 z2o0_00a = TRR((LAS char*)(w2a) + 0);
    bf16x4 z2o0_00b = TRR((LAS char*)(w2a) + 512); bf16x8 z2o0_00 = CAT(z2o0_00a,z2o0_00b);
    u32x4 A0n3_1; LDV(A0n3_1, l16, hn3, 1);
    SB;
    ap1 = MFMA(BC(A00_0), z0o0_01, ap1); PIN1(ap1);
    bf16x4 z2o0_01a = TRR((LAS char*)(w2b) + 0);
    bf16x4 z2o0_01b = TRR((LAS char*)(w2b) + 512); bf16x8 z2o0_01 = CAT(z2o0_01a,z2o0_01b);
    unsigned wm0 = sb + w * 8192; asm volatile("" : "+s"(wm0));
    SB;
    ap0 = MFMA(BC(A00_1), z0o0_10, ap0); PIN1(ap0);
    bf16x4 z2o0_10a = TRR((LAS char*)(w2a) + 2048);
    bf16x4 z2o0_10b = TRR((LAS char*)(w2a) + 2560); bf16x8 z2o0_10 = CAT(z2o0_10a,z2o0_10b);
    u32x4 A2n1_0; LDV(A2n1_0, l16r, hn1, 0);
    SB;
    ap1 = MFMA(BC(A00_1), z0o0_11, ap1); PIN1(ap1);
    bf16x4 z2o0_11a = TRR((LAS char*)(w2b) + 2048);
    bf16x4 z2o0_11b = TRR((LAS char*)(w2b) + 2560); bf16x8 z2o0_11 = CAT(z2o0_11a,z2o0_11b);
    unsigned ra0 = sb + mu * 8192 + (w & 3) * 1024 + (st >> 3) * 4096 + lane * 16, ra1 = ra0 ^ 128; asm volatile("" : "+v"(ra0), "+v"(ra1));
    SB;
    ap0 = MFMA(BC(A20_0), z2o0_00, ap0); PIN1(ap0);
    SB;
    asm volatile("s_waitcnt vmcnt(15)\n\ts_barrier" ::: "memory");
    unsigned w2c = w2z + kpo + 65536; PIN1(w2c);
    bf16x4 z0n1_00a = TRR((LAS char*)(w2c) + 0);
    bf16x4 z0n1_00b = TRR((LAS char*)(w2c) + 512); bf16x8 z0n1_00 = CAT(z0n1_00a,z0n1_00b);
    u32x4 A2n1_1; LDV(A2n1_1, l16r, hn1, 1);
    SB;
    ap1 = MFMA(BC(A20_0), z2o0_01, ap1); PIN1(ap1);
    unsigned w2d = (w2z ^ 64) + kpo + 65536; PIN1(w2d);
    bf16x4 z0n1_01a = TRR((LAS char*)(w2d) + 0);
    bf16x4 z0n1_01b = TRR((LAS char*)(w2d) + 512); bf16x8 z0n1_01 = CAT(z0n1_01a,z0n1_01b);
    SB;
    ap0 = MFMA(BC(A20_1), z2o0_10, ap0); PIN1(ap0);
    bf16x4 z0n1_10a = TRR((LAS char*)(w2c) + 2048);
    bf16x4 z0n1_10b = TRR((LAS char*)(w2c) + 2560); bf16x8 z0n1_10 = CAT(z0n1_10a,z0n1_10b);
    u32x4 A2n2_0; LDV(A2n2_0, l16r, hn2, 0);
    SB;
    ap1 = MFMA(BC(A20_1), z2o0_11, ap1); PIN1(ap1);
    bf16x4 z0n1_11a = TRR((LAS char*)(w2d) + 2048);
    bf16x4 z0n1_11b = TRR((LAS char*)(w2d) + 2560); bf16x8 z0n1_11 = CAT(z0n1_11a,z0n1_11b);
    SB;
    asm volatile("s_waitcnt vmcnt(14)" : "+v"(A0n1_0));
    if (!keep) { an0 = MFMA(BC(A0n1_0), z0n1_00, ap0); asm volatile("" : "+v"(an0)); } else { an0 = MFMA(BC(A0n1_0), z0n1_00, zero16); }
    w2c += 16384; PIN1(w2c);
    bf16x4 z0n2_00a = TRR((LAS char*)(w2c) + 49152);
    bf16x4 z0n2_00b = TRR((LAS char*)(w2c) + 49664); bf16x8 z0n2_00 = CAT(z0n2_00a,z0n2_00b);
    u32x4 A2n2_1; LDV(A2n2_1, l16r, hn2, 1);
    SB;
    if (!keep) { an1 = MFMA(BC(A0n1_0), z0n1_01, ap1); asm volatile("" : "+v"(an1)); } else { an1 = MFMA(BC(A0n1_0), z0n1_01, zero16); }
    w2d += 16384; PIN1(w2d);
    bf16x4 z0n2_01a = TRR((LAS char*)(w2d) + 49152);
    bf16x4 z0n2_01b = TRR((LAS char*)(w2d) + 49664); bf16x8 z0n2_01 = CAT(z0n2_01a,z0n2_01b);
    SB;
    asm volatile("s_waitcnt vmcnt(14)" : "+v"(A0n1_1));
    an0 = MFMA(BC(A0n1_1), z0n1_10, an0); PIN1(an0);
    bf16x4 z0n2_10a = TRR((LAS char*)(w2c) + 51200);
    bf16x4 z0n2_10b = TRR((LAS char*)(w2c) + 51712); bf16x8 z0n2_10 = CAT(z0n2_10a,z0n2_10b);
    u32x4 A2n3_0; LDV(A2n3_0, l16r, hn3, 0);
    SB;
    an1 = MFMA(BC(A0n1_1), z0n1_11, an1); PIN1(an1);
    bf16x4 z0n2_11a = TRR((LAS char*)(w2d) + 51200);
    bf16x4 z0n2_11b = TRR((LAS char*)(w2d) + 51712); bf16x8 z0n2_11 = CAT(z0n2_11a,z0n2_11b);
    SB;
    asm volatile("s_waitcnt vmcnt(14)" : "+v"(A0n2_0));
    an0 = MFMA(BC(A0n2_0), z0n2_00, an0); PIN1(an0);
    bf16x4 z0n3_00a = TRR((LAS char*)(w2c) + 0);
    bf16x4 z0n3_00b = TRR((LAS char*)(w2c) + 512); bf16x8 z0n3_00 = CAT(z0n3_00a,z0n3_00b);
    u32x4 A2n3_1; LDV(A2n3_1, l16r, hn3, 1);
    SB;
    an1 = MFMA(BC(A0n2_0), z0n2_01, an1); PIN1(an1);
    bf16x4 z0n3_01a = TRR((LAS char*)(w2d) + 0);
    bf16x4 z0n3_01b = TRR((LAS char*)(w2d) + 512); bf16x8 z0n3_01 = CAT(z0n3_01a,z0n3_01b);
    SB;
    asm volatile("s_waitcnt vmcnt(13)" : "+v"(A0n2_1));
    an0 = MFMA(BC(A0n2_1), z0n2_10, an0); PIN1(an0);
    bf16x4 z0n3_10a = TRR((LAS char*)(w2c) + 2048);
    bf16x4 z0n3_10b = TRR((LAS char*)(w2c) + 2560); bf16x8 z0n3_10 = CAT(z0n3_10a,z0n3_10b);
    u32x4 A0k1_0; LDV(A0k1_0, l16, hk1, 0);
    SB;
    an1 = MFMA(BC(A0n2_1), z0n2_11, an1); PIN1(an1);
    bf16x4 z0n3_11a = TRR((LAS char*)(w2d) + 2048);
    bf16x4 z0n3_11b = TRR((LAS char*)(w2d) + 2560); bf16x8 z0n3_11 = CAT(z0n3_11a,z0n3_11b);
    SB;
    asm volatile("s_waitcnt vmcnt(13)" : "+v"(A0n3_0));
    an0 = MFMA(BC(A0n3_0), z0n3_00, an0); PIN1(an0);
    SB;
    asm volatile("s_waitcnt vmcnt(8)\n\ts_barrier" ::: "memory");
    bf16x4 z2n1_00a = TRR((LAS char*)(w2c) + 16384);
    bf16x4 z2n1_00b = TRR((LAS char*)(w2c) + 16896); bf16x8 z2n1_00 = CAT(z2n1_00a,z2n1_00b);
    u32x4 A0k1_1; LDV(A0k1_1, l16, hk1, 1);
    SB;
    an1 = MFMA(BC(A0n3_0), z0n3_01, an1); PIN1(an1);
    bf16x4 z2n1_10a = TRR((LAS char*)(w2c) + 18432);
    bf16x4 z2n1_10b = TRR((LAS char*)(w2c) + 18944); bf16x8 z2n1_10 = CAT(z2n1_10a,z2n1_10b);
    SB;
    asm volatile("s_waitcnt vmcnt(8)" : "+v"(A0n3_1));
    an0 = MFMA(BC(A0n3_1), z0n3_10, an0); PIN1(an0);
    bf16x4 z2n2_00a = TRR((LAS char*)(w2c) + 32768);
    bf16x4 z2n2_00b = TRR((LAS char*)(w2c) + 33280); bf16x8 z2n2_00 = CAT(z2n2_00a,z2n2_00b);
    u32x4 A0k2_0; LDV(A0k2_0, l16, hk2, 0);
    asm volatile("s_waitcnt vmcnt(7)" : "+v"(A2n1_0), "+v"(A2n1_1));
    if (__builtin_expect(ANY(max(max(A2n1_0[0], A2n1_0[2]), max(A2n1_1[0], A2n1_1[2])) == ~0u), 0)) { for (int sp = 0; sp < SPINMAX; ++sp) { __builtin_amdgcn_s_sleep(1); LDV(A2n1_0, l16r, hn1, 0); LDV(A2n1_1, l16r, hn1, 1); asm volatile("s_waitcnt vmcnt(0)" : "+v"(A2n1_0), "+v"(A2n1_1)); if (!(ANY(max(max(A2n1_0[0], A2n1_0[2]), max(A2n1_1[0], A2n1_1[2])) == ~0u))) break; } }
    SB;
    an1 = MFMA(BC(A0n3_1), z0n3_11, an1); PIN1(an1);
    bf16x4 z2n2_10a = TRR((LAS char*)(w2c) + 34816);
    bf16x4 z2n2_10b = TRR((LAS char*)(w2c) + 35328); bf16x8 z2n2_10 = CAT(z2n2_10a,z2n2_10b);
    SB;
    an0 = MFMA(BC(A2n1_0), z2n1_00, an0); PIN1(an0);
    w2c += 65536; PIN1(w2c);
    bf16x4 z2n3_00a = TRR((LAS char*)(w2c) + 0);
    bf16x4 z2n3_00b = TRR((LAS char*)(w2c) + 512); bf16x8 z2n3_00 = CAT(z2n3_00a,z2n3_00b);
    u32x4 A0k2_1; LDV(A0k2_1, l16, hk2, 1);
    asm volatile("s_waitcnt vmcnt(6)" : "+v"(A2n2_0), "+v"(A2n2_1));
    if (__builtin_expect(ANY(max(max(A2n2_0[0], A2n2_0[2]), max(A2n2_1[0], A2n2_1[2])) == ~0u), 0)) { for (int sp = 0; sp < SPINMAX; ++sp) { __builtin_amdgcn_s_sleep(1); LDV(A2n2_0, l16r, hn2, 0); LDV(A2n2_1, l16r, hn2, 1); asm volatile("s_waitcnt vmcnt(0)" : "+v"(A2n2_0), "+v"(A2n2_1)); if (!(ANY(max(max(A2n2_0[0], A2n2_0[2]), max(A2n2_1[0], A2n2_1[2])) == ~0u))) break; } }
    SB;
    an0 = MFMA(BC(A2n1_1), z2n1_10, an0); PIN1(an0);
    bf16x4 z2n3_10a = TRR((LAS char*)(w2c) + 2048);
    bf16x4 z2n3_10b = TRR((LAS char*)(w2c) + 2560); bf16x8 z2n3_10 = CAT(z2n3_10a,z2n3_10b);
    SB;
    an0 = MFMA(BC(A2n2_0), z2n2_00, an0); PIN1(an0);
    bf16x4 z2n1_01a = TRR((LAS char*)(w2d) + 16384);
    bf16x4 z2n1_01b = TRR((LAS char*)(w2d) + 16896); bf16x8 z2n1_01 = CAT(z2n1_01a,z2n1_01b);
    u32x4 A0k3_0; LDV(A0k3_0, l16, hk3, 0);
    asm volatile("s_waitcnt vmcnt(5)" : "+v"(A2n3_0), "+v"(A2n3_1));
    if (__builtin_expect(ANY(max(max(A2n3_0[0], A2n3_0[2]), max(A2n3_1[0], A2n3_1[2])) == ~0u), 0)) { for (int sp = 0; sp < SPINMAX; ++sp) { __builtin_amdgcn_s_sleep(1); LDV(A2n3_0, l16r, hn3, 0); LDV(A2n3_1, l16r, hn3, 1); asm volatile("s_waitcnt vmcnt(0)" : "+v"(A2n3_0), "+v"(A2n3_1)); if (!(ANY(max(max(A2n3_0[0], A2n3_0[2]), max(A2n3_1[0], A2n3_1[2])) == ~0u))) break; } }
    SB;
    an0 = MFMA(BC(A2n2_1), z2n2_10, an0); PIN1(an0);
    bf16x4 z2n1_11a = TRR((LAS char*)(w2d) + 18432);
    bf16x4 z2n1_11b = TRR((LAS char*)(w2d) + 18944); bf16x8 z2n1_11 = CAT(z2n1_11a,z2n1_11b);
    SB;
    an0 = MFMA(BC(A2n3_0), z2n3_00, an0); PIN1(an0);
    bf16x4 z2n2_01a = TRR((LAS char*)(w2d) + 32768);
    bf16x4 z2n2_01b = TRR((LAS char*)(w2d) + 33280); bf16x8 z2n2_01 = CAT(z2n2_01a,z2n2_01b);
    u32x4 A0k3_1; LDV(A0k3_1, l16, hk3, 1);
    SB;
    an0 = MFMA(BC(A2n3_1), z2n3_10, an0); PIN1(an0);
    bf16x4 z2n2_11a = TRR((LAS char*)(w2d) + 34816);
    bf16x4 z2n2_11b = TRR((LAS char*)(w2d) + 35328); bf16x8 z2n2_11 = CAT(z2n2_11a,z2n2_11b);
    SB;
    an1 = MFMA(BC(A2n1_0), z2n1_01, an1); PIN1(an1);
    w2d += 65536; PIN1(w2d);
    bf16x4 z2n3_01a = TRR((LAS char*)(w2d) + 0);
    bf16x4 z2n3_01b = TRR((LAS char*)(w2d) + 512); bf16x8 z2n3_01 = CAT(z2n3_01a,z2n3_01b);
    bf16x4 z2n3_11a = TRR((LAS char*)(w2d) + 2048);
    bf16x4 z2n3_11b = TRR((LAS char*)(w2d) + 2560); bf16x8 z2n3_11 = CAT(z2n3_11a,z2n3_11b);
    u32x4 A2k1_0; LDV(A2k1_0, l16r, hk1, 0);
    SB;
    an1 = MFMA(BC(A2n1_1), z2n1_11, an1); PIN1(an1);
    SB; asm volatile("" :: "v"(z2n3_11)); SB;
    WROW(an0, 0, 0, "s_mov_b32 m0, %4\n\ts_nop 0");
    SB;
    an1 = MFMA(BC(A2n2_0), z2n2_01, an1); PIN1(an1);
    unsigned w2e = w2z + (8192 - kpo) + 65536; PIN1(w2e);
    bf16x4 z0k1_00a = TRR((LAS char*)(w2e) + 0);
    bf16x4 z0k1_00b = TRR((LAS char*)(w2e) + 512); bf16x8 z0k1_00 = CAT(z0k1_00a,z0k1_00b);
    u32x4 A2k1_1; LDV(A2k1_1, l16r, hk1, 1);
    WROW(an0, 1, 1024, "s_mov_b32 m0, %4\n\ts_nop 0");
    SB;
    an1 = MFMA(BC(A2n2_1), z2n2_11, an1); PIN1(an1);
    unsigned w2f = (w2z ^ 64) + (8192 - kpo) + 65536; PIN1(w2f);
    bf16x4 z0k1_01a = TRR((LAS char*)(w2f) + 0);
    bf16x4 z0k1_01b = TRR((LAS char*)(w2f) + 512); bf16x8 z0k1_01 = CAT(z0k1_01a,z0k1_01b);
    WROW(an0, 2, 2048, "s_mov_b32 m0, %4\n\ts_nop 0");
    SB;
    an1 = MFMA(BC(A2n3_0), z2n3_01, an1); PIN1(an1);
    bf16x4 z0k1_10a = TRR((LAS char*)(w2e) + 2048);
    bf16x4 z0k1_10b = TRR((LAS char*)(w2e) + 2560); bf16x8 z0k1_10 = CAT(z0k1_10a,z0k1_10b);
    u32x4 A2k2_0; LDV(A2k2_0, l16r, hk2, 0);
    WROW(an0, 3, 3072, "s_mov_b32 m0, %4\n\ts_nop 0");
    SB;
    an1 = MFMA(BC(A2n3_1), z2n3_11, an1); PIN1(an1);
    bf16x4 z0k1_11a = TRR((LAS char*)(w2f) + 2048);
    bf16x4 z0k1_11b = TRR((LAS char*)(w2f) + 2560); bf16x8 z0k1_11 = CAT(z0k1_11a,z0k1_11b);
    SB;
    asm volatile("s_waitcnt vmcnt(8)" : "+v"(A0k1_0));
    MFSEL(ap0, BC(A0k1_0), z0k1_00); aq0 = ap0;
    w2e += 16384; PIN1(w2e);
    bf16x4 z0k2_00a = TRR((LAS char*)(w2e) + 49152);
    bf16x4 z0k2_00b = TRR((LAS char*)(w2e) + 49664); bf16x8 z0k2_00 = CAT(z0k2_00a,z0k2_00b);
    u32x4 A2k2_1; LDV(A2k2_1, l16r, hk2, 1);
    SB;
    MFSEL(ap1, BC(A0k1_0), z0k1_01); aq1 = ap1;
    SB; asm volatile("" :: "v"(z0k2_00)); SB;
    w2f += 16384; PIN1(w2f);
    bf16x4 z0k2_01a = TRR((LAS char*)(w2f) + 49152);
    bf16x4 z0k2_01b = TRR((LAS char*)(w2f) + 49664); bf16x8 z0k2_01 = CAT(z0k2_01a,z0k2_01b);
    WROW(an1, 0, 4096, "s_mov_b32 m0, %4\n\ts_nop 0");
    WROW(an1, 1, 5120, "s_mov_b32 m0, %4\n\ts_nop 0");
    SB;
    asm volatile("s_waitcnt vmcnt(8)" : "+v"(A0k1_1));
    aq0 = MFMA(BC(A0k1_1), z0k1_10, aq0); PIN1(aq0);
    bf16x4 z0k2_10a = TRR((LAS char*)(w2e) + 51200);
    bf16x4 z0k2_10b = TRR((LAS char*)(w2e) + 51712); bf16x8 z0k2_10 = CAT(z0k2_10a,z0k2_10b);
    u32x4 A2k3_0; LDV(A2k3_0, l16r, hk3, 0);
    WROW(an1, 2, 6144, "s_mov_b32 m0, %4\n\ts_nop 0");
    SB;
    aq1 = MFMA(BC(A0k1_1), z0k1_11, aq1); PIN1(aq1);
    bf16x4 z0k2_11a = TRR((LAS char*)(w2f) + 51200);
    bf16x4 z0k2_11b = TRR((LAS char*)(w2f) + 51712); bf16x8 z0k2_11 = CAT(z0k2_11a,z0k2_11b);
    WROW(an1, 3, 7168, "s_mov_b32 m0, %4\n\ts_nop 0");
    SB;
    asm volatile("s_waitcnt vmcnt(8)" : "+v"(A0k2_0));
    aq0 = MFMA(BC(A0k2_0), z0k2_00, aq0); PIN1(aq0);
    SB;
    asm volatile("s_waitcnt lgkmcnt(0)\n\ts_barrier" ::: "memory");
    bf16x4 z0k3_00a = TRR((LAS char*)(w2e) + 0);
    bf16x4 z0k3_00b = TRR((LAS char*)(w2e) + 512); bf16x8 z0k3_00 = CAT(z0k3_00a,z0k3_00b);
    f32x4 pa00 = *(LAS f32x4*)((LAS char*)(ra0) + 0); f32x4 pa01 = *(LAS f32x4*)((LAS char*)(ra0) + 16384); f32x4 pa02 = *(LAS f32x4*)((LAS char*)(ra0) + 32768); f32x4 pa03 = *(LAS f32x4*)((LAS char*)(ra0) + 49152);
    u32x4 A2k3_1; LDV(A2k3_1, l16r, hk3, 1);
    SB;
    aq1 = MFMA(BC(A0k2_0), z0k2_01, aq1); PIN1(aq1);
    bf16x4 z0k3_01a = TRR((LAS char*)(w2f) + 0);
    bf16x4 z0k3_01b = TRR((LAS char*)(w2f) + 512); bf16x8 z0k3_01 = CAT(z0k3_01a,z0k3_01b);
    f32x4 pa10 = *(LAS f32x4*)((LAS char*)(ra1) + 0); f32x4 pa11 = *(LAS f32x4*)((LAS char*)(ra1) + 16384); f32x4 pa12 = *(LAS f32x4*)((LAS char*)(ra1) + 32768); f32x4 pa13 = *(LAS f32x4*)((LAS char*)(ra1) + 49152);
    SB;
    asm volatile("s_waitcnt vmcnt(8)" : "+v"(A0k2_1));
    aq0 = MFMA(BC(A0k2_1), z0k2_10, aq0); PIN1(aq0);
    bf16x4 z0k3_10a = TRR((LAS char*)(w2e) + 2048);
    bf16x4 z0k3_10b = TRR((LAS char*)(w2e) + 2560); bf16x8 z0k3_10 = CAT(z0k3_10a,z0k3_10b);
    pa00 = f32x4{fadd1(fadd1(pa00[0],pa01[0]), fadd1(pa02[0],pa03[0])), fadd1(fadd1(pa00[1],pa01[1]), fadd1(pa02[1],pa03[1])), fadd1(fadd1(pa00[2],pa01[2]), fadd1(pa02[2],pa03[2])), fadd1(fadd1(pa00[3],pa01[3]), fadd1(pa02[3],pa03[3]))}; STX(pa00, pso, 0);
    SB;
    aq1 = MFMA(BC(A0k2_1), z0k2_11, aq1); PIN1(aq1);
    bf16x4 z0k3_11a = TRR((LAS char*)(w2f) + 2048);
    bf16x4 z0k3_11b = TRR((LAS char*)(w2f) + 2560); bf16x8 z0k3_11 = CAT(z0k3_11a,z0k3_11b);
    pa10 = f32x4{fadd1(fadd1(pa10[0],pa11[0]), fadd1(pa12[0],pa13[0])), fadd1(fadd1(pa10[1],pa11[1]), fadd1(pa12[1],pa13[1])), fadd1(fadd1(pa10[2],pa11[2]), fadd1(pa12[2],pa13[2])), fadd1(fadd1(pa10[3],pa11[3]), fadd1(pa12[3],pa13[3]))}; STX(pa10, pso, 1);
    SB;
    asm volatile("s_waitcnt vmcnt(9)" : "+v"(A0k3_0));
    aq0 = MFMA(BC(A0k3_0), z0k3_00, aq0); PIN1(aq0);
    bf16x4 z2k1_00a = TRR((LAS char*)(w2e) + 16384);
    bf16x4 z2k1_00b = TRR((LAS char*)(w2e) + 16896); bf16x8 z2k1_00 = CAT(z2k1_00a,z2k1_00b);
    SB;
    aq1 = MFMA(BC(A0k3_0), z0k3_01, aq1); PIN1(aq1);
    bf16x4 z2k1_10a = TRR((LAS char*)(w2e) + 18432);
    bf16x4 z2k1_10b = TRR((LAS char*)(w2e) + 18944); bf16x8 z2k1_10 = CAT(z2k1_10a,z2k1_10b);
    SB;
    asm volatile("s_waitcnt vmcnt(8)" : "+v"(A0k3_1));
    aq0 = MFMA(BC(A0k3_1), z0k3_10, aq0); PIN1(aq0);
    bf16x4 z2k2_00a = TRR((LAS char*)(w2e) + 32768);
    bf16x4 z2k2_00b = TRR((LAS char*)(w2e) + 33280); bf16x8 z2k2_00 = CAT(z2k2_00a,z2k2_00b);
    asm volatile("s_waitcnt vmcnt(6)" : "+v"(A2k1_0), "+v"(A2k1_1));
    if (__builtin_expect(ANY(max(max(A2k1_0[0], A2k1_0[2]), max(A2k1_1[0], A2k1_1[2])) == ~0u), 0)) { for (int sp = 0; sp < SPINMAX; ++sp) { __builtin_amdgcn_s_sleep(1); LDV(A2k1_0, l16r, hk1, 0); LDV(A2k1_1, l16r, hk1, 1); asm volatile("s_waitcnt vmcnt(0)" : "+v"(A2k1_0), "+v"(A2k1_1)); if (!(ANY(max(max(A2k1_0[0], A2k1_0[2]), max(A2k1_1[0], A2k1_1[2])) == ~0u))) break; } }
    SB;
    aq1 = MFMA(BC(A0k3_1), z0k3_11, aq1); PIN1(aq1);
    bf16x4 z2k2_10a = TRR((LAS char*)(w2e) + 34816);
    bf16x4 z2k2_10b = TRR((LAS char*)(w2e) + 35328); bf16x8 z2k2_10 = CAT(z2k2_10a,z2k2_10b);
    SB;
    aq0 = MFMA(BC(A2k1_0), z2k1_00, aq0); PIN1(aq0);
    SB;
    asm volatile("s_barrier" ::: "memory");
    w2e += 65536; PIN1(w2e);
    bf16x4 z2k3_00a = TRR((LAS char*)(w2e) + 0);
    bf16x4 z2k3_00b = TRR((LAS char*)(w2e) + 512); bf16x8 z2k3_00 = CAT(z2k3_00a,z2k3_00b);
    asm volatile("s_waitcnt vmcnt(4)" : "+v"(A2k2_0), "+v"(A2k2_1));
    if (__builtin_expect(ANY(max(max(A2k2_0[0], A2k2_0[2]), max(A2k2_1[0], A2k2_1[2])) == ~0u), 0)) { for (int sp = 0; sp < SPINMAX; ++sp) { __builtin_amdgcn_s_sleep(1); LDV(A2k2_0, l16r, hk2, 0); LDV(A2k2_1, l16r, hk2, 1); asm volatile("s_waitcnt vmcnt(0)" : "+v"(A2k2_0), "+v"(A2k2_1)); if (!(ANY(max(max(A2k2_0[0], A2k2_0[2]), max(A2k2_1[0], A2k2_1[2])) == ~0u))) break; } }
    SB;
    aq0 = MFMA(BC(A2k1_1), z2k1_10, aq0); PIN1(aq0);
    bf16x4 z2k3_10a = TRR((LAS char*)(w2e) + 2048);
    bf16x4 z2k3_10b = TRR((LAS char*)(w2e) + 2560); bf16x8 z2k3_10 = CAT(z2k3_10a,z2k3_10b);
    SB;
    aq0 = MFMA(BC(A2k2_0), z2k2_00, aq0); PIN1(aq0);
    bf16x4 z2k1_01a = TRR((LAS char*)(w2f) + 16384);
    bf16x4 z2k1_01b = TRR((LAS char*)(w2f) + 16896); bf16x8 z2k1_01 = CAT(z2k1_01a,z2k1_01b);
    asm volatile("s_waitcnt vmcnt(2)" : "+v"(A2k3_0), "+v"(A2k3_1));
    if (__builtin_expect(ANY(max(max(A2k3_0[0], A2k3_0[2]), max(A2k3_1[0], A2k3_1[2])) == ~0u), 0)) { for (int sp = 0; sp < SPINMAX; ++sp) { __builtin_amdgcn_s_sleep(1); LDV(A2k3_0, l16r, hk3, 0); LDV(A2k3_1, l16r, hk3, 1); asm volatile("s_waitcnt vmcnt(0)" : "+v"(A2k3_0), "+v"(A2k3_1)); if (!(ANY(max(max(A2k3_0[0], A2k3_0[2]), max(A2k3_1[0], A2k3_1[2])) == ~0u))) break; } }
    SB;
    aq0 = MFMA(BC(A2k2_1), z2k2_10, aq0); PIN1(aq0);
    bf16x4 z2k1_11a = TRR((LAS char*)(w2f) + 18432);
    bf16x4 z2k1_11b = TRR((LAS char*)(w2f) + 18944); bf16x8 z2k1_11 = CAT(z2k1_11a,z2k1_11b);
    SB;
    aq0 = MFMA(BC(A2k3_0), z2k3_00, aq0); PIN1(aq0);
    bf16x4 z2k2_01a = TRR((LAS char*)(w2f) + 32768);
    bf16x4 z2k2_01b = TRR((LAS char*)(w2f) + 33280); bf16x8 z2k2_01 = CAT(z2k2_01a,z2k2_01b);
    SB;
    aq0 = MFMA(BC(A2k3_1), z2k3_10, aq0); PIN1(aq0);
    bf16x4 z2k2_11a = TRR((LAS char*)(w2f) + 34816);
    bf16x4 z2k2_11b = TRR((LAS char*)(w2f) + 35328); bf16x8 z2k2_11 = CAT(z2k2_11a,z2k2_11b);
    SB;
    aq1 = MFMA(BC(A2k1_0), z2k1_01, aq1); PIN1(aq1);
    w2f += 65536; PIN1(w2f);
    bf16x4 z2k3_01a = TRR((LAS char*)(w2f) + 0);
    bf16x4 z2k3_01b = TRR((LAS char*)(w2f) + 512); bf16x8 z2k3_01 = CAT(z2k3_01a,z2k3_01b);
    bf16x4 z2k3_11a = TRR((LAS char*)(w2f) + 2048);
    bf16x4 z2k3_11b = TRR((LAS char*)(w2f) + 2560); bf16x8 z2k3_11 = CAT(z2k3_11a,z2k3_11b);
    SB;
    aq1 = MFMA(BC(A2k1_1), z2k1_11, aq1); PIN1(aq1);
    SB; asm volatile("" :: "v"(z2k3_11)); SB;
    WROW(aq0, 0, 0, "s_mov_b32 m0, %4\n\ts_nop 0");
    SB;
    aq1 = MFMA(BC(A2k2_0), z2k2_01, aq1); PIN1(aq1);
    WROW(aq0, 1, 1024, "s_mov_b32 m0, %4\n\ts_nop 0");
    SB;
    aq1 = MFMA(BC(A2k2_1), z2k2_11, aq1); PIN1(aq1);
    u32x4 rm0; LDA(rm0, pli, 0);
    WROW(aq0, 2, 2048, "s_mov_b32 m0, %4\n\ts_nop 0");
    SB;
    aq1 = MFMA(BC(A2k3_0), z2k3_01, aq1); PIN1(aq1);
    u32x4 rm1; LDA(rm1, pli, 1);
    WROW(aq0, 3, 3072, "s_mov_b32 m0, %4\n\ts_nop 0");
    SB;
    aq1 = MFMA(BC(A2k3_1), z2k3_11, aq1); PIN1(aq1);
    SB;
    WROW(aq1, 0, 4096, "s_mov_b32 m0, %4\n\ts_nop 10");
    WROW(aq1, 1, 5120, "");
    WROW(aq1, 2, 6144, "");
    WROW(aq1, 3, 7168, "");
    asm volatile("s_waitcnt lgkmcnt(0)\n\ts_barrier" ::: "memory");
    f32x4 fr00 = *(LAS f32x4*)((LAS char*)(ra0) + 0); f32x4 fr01 = *(LAS f32x4*)((LAS char*)(ra0) + 16384); f32x4 fr02 = *(LAS f32x4*)((LAS char*)(ra0) + 32768); f32x4 fr03 = *(LAS f32x4*)((LAS char*)(ra0) + 49152);
    f32x4 fr10 = *(LAS f32x4*)((LAS char*)(ra1) + 0); f32x4 fr11 = *(LAS f32x4*)((LAS char*)(ra1) + 16384); f32x4 fr12 = *(LAS f32x4*)((LAS char*)(ra1) + 32768); f32x4 fr13 = *(LAS f32x4*)((LAS char*)(ra1) + 49152);
    SB;
    asm volatile("" :: "v"(aq1)); SB;
    f32x4 o0 = ((fr00 + fr01) + (fr02 + fr03)) + sk0;
    f32x4 o1 = ((fr10 + fr11) + (fr12 + fr13)) + sk1;
    PIN2(o0, o1);
    asm volatile("s_waitcnt vmcnt(0)" : "+v"(rm0), "+v"(rm1));
    if (__builtin_expect(ANY(max(max(rm0[0], rm0[2]), max(rm1[0], rm1[2])) == ~0u), 0)) { for (int sp = 0; sp < SPINMAX; ++sp) { __builtin_amdgcn_s_sleep(1); LDA(rm0, pli, 0); LDA(rm1, pli, 1); asm volatile("s_waitcnt vmcnt(0)" : "+v"(rm0), "+v"(rm1)); if (!(ANY(max(max(rm0[0], rm0[2]), max(rm1[0], rm1[2])) == ~0u))) break; } }
    o0 += __builtin_bit_cast(f32x4, rm0); o1 += __builtin_bit_cast(f32x4, rm1);
    asm volatile("buffer_store_dwordx4 %0, %2, %3, 0 offen sc1\n\tbuffer_store_dwordx4 %1, %4, %3, 0 offen sc1\n\ts_nop 1" :: "v"(o0), "v"(o1), "v"(vo2), "s"(rso), "v"(vo3) : "memory");
  }
}

#define BD(p, bytes) (const void*)(p), (unsigned)(bytes), 0x00020000u

extern "C" void kernel_launch(void* const* d_in, const int* in_sizes, int n_in, void* d_out, int out_size, void* d_ws, size_t ws_size, hipStream_t stream) {
  fused_kernel<<<256, 512, 0, stream>>>(
      BD(d_in[0], 2048 * 896), BD(d_in[10], 448 * 3584), BD(d_in[11], 448 * 3584), BD(d_ws, WS_END),
      BD(d_in[1], 896), BD(d_in[2], 896), BD(d_in[7], 896), BD(d_in[12], 1792 * 896),
      BD(d_in[3], 448 * 896), BD(d_in[4], 448 * 896), BD(d_in[5], 448 * 896), BD(d_in[6], 448 * 896),
      BD(d_in[8], 896), BD(d_out, 2048 * 1792), BD(d_in[13], 896));
}
```
